```python
import torch

def _softmax(x, axis=-1):
    return torch.softmax(x, dim=axis)
def _gelu(x):
    return torch.nn.functional.gelu(x, approximate="tanh")
def _silu(x):
    return torch.nn.functional.silu(x)
def _sigmoid(x):
    return torch.sigmoid(x)
def _layernorm(x, axis=-1, eps=1e-5):
    m = x.mean(dim=axis, keepdim=True)
    v = x.var(dim=axis, keepdim=True, unbiased=False)
    return (x - m) / torch.sqrt(v + eps)
def _scatter_add(tgt, idx, src, axis=0):
    return tgt.clone().index_add_(axis, idx, src)
def _segment_sum(src, seg, n):
    out = torch.zeros((n,) + src.shape[1:], dtype=src.dtype)
    return out.index_add_(0, seg, src)
def _pad(x, pw):
    flat = tuple(v for pair in reversed(pw) for v in pair)
    return torch.nn.functional.pad(x, flat)

kernel_name = 'concat_heads__mha_block_cached__mha_block_cached__near_ridge'

def setup_inputs():

    _w = (lambda a: (lambda t: t.to(torch.bfloat16) if t.is_floating_point() else t)(torch.as_tensor(a)))
    def _A_setup():
        B, Sq, D = 8, 160, 448
        H, Sk, Dh = 16, 1024, 256
        return {
            "x": _w((lambda *s: torch.randn(*s, dtype=torch.bfloat16) * 0.8)(B, Sq, D)),
            "k_cache": _w((lambda *s: torch.randn(*s, dtype=torch.bfloat16) * 0.8)(B, H, Sk, Dh)),
            "v_cache": _w((lambda *s: torch.randn(*s, dtype=torch.bfloat16) * 0.05)(B, H, Sk, Dh)),
            "wq": _w((lambda *s: torch.randn(*s, dtype=torch.bfloat16) * 0.8)(D, H * Dh) * (D ** -0.5)),
            "wo": _w((lambda *s: torch.randn(*s, dtype=torch.bfloat16) * 0.05)(H * Dh, D) * ((H * Dh) ** -0.5)),
        }
    _A_in = _A_setup()

    def _B_setup():
        B, Sq, D = 8, 160, 448
        H, Sk, Dh = 16, 1024, 256
        return {
            "x": _w((lambda *s: torch.randn(*s, dtype=torch.bfloat16) * 0.8)(B, Sq, D)),
            "k_cache": _w((lambda *s: torch.randn(*s, dtype=torch.bfloat16) * 0.8)(B, H, Sk, Dh)),
            "v_cache": _w((lambda *s: torch.randn(*s, dtype=torch.bfloat16) * 0.05)(B, H, Sk, Dh)),
            "wq": _w((lambda *s: torch.randn(*s, dtype=torch.bfloat16) * 0.8)(D, H * Dh) * (D ** -0.5)),
            "wo": _w((lambda *s: torch.randn(*s, dtype=torch.bfloat16) * 0.05)(H * Dh, D) * ((H * Dh) ** -0.5)),
        }
    _B_in = _B_setup()

    return {
        "A_x": _A_in["x"],
        "A_k_cache": _A_in["k_cache"],
        "A_v_cache": _A_in["v_cache"],
        "A_wq": _A_in["wq"],
        "A_wo": _A_in["wo"],
        "B_x": _B_in["x"],
        "B_k_cache": _B_in["k_cache"],
        "B_v_cache": _B_in["v_cache"],
        "B_wq": _B_in["wq"],
        "B_wo": _B_in["wo"],
    }

def reference(A_x, A_k_cache, A_v_cache, A_wq, A_wo, B_x, B_k_cache, B_v_cache, B_wq, B_wo):
    def _A_ref(x, k_cache, v_cache, wq, wo):
        B, Sq, D = x.shape
        H, Dh = 16, 256
        q = (lambda x, *d: x.permute(*d))((x @ wq).reshape(B, Sq, H, Dh), 0, 2, 1, 3)
        scores = torch.einsum("bhqd,bhkd->bhqk", q, k_cache) * (Dh ** -0.5)
        p = _softmax(scores, axis=-1)
        o = torch.einsum("bhqk,bhkd->bhqd", p, v_cache)
        return (lambda x, *d: x.permute(*d))(o, 0, 2, 1, 3).reshape(B, Sq, H * Dh) @ wo

    def _B_ref(x, k_cache, v_cache, wq, wo):
        B, Sq, D = x.shape
        H, Dh = 16, 256
        q = (lambda x, *d: x.permute(*d))((x @ wq).reshape(B, Sq, H, Dh), 0, 2, 1, 3)
        scores = torch.einsum("bhqd,bhkd->bhqk", q, k_cache) * (Dh ** -0.5)
        p = _softmax(scores, axis=-1)
        o = torch.einsum("bhqk,bhkd->bhqd", p, v_cache)
        return (lambda x, *d: x.permute(*d))(o, 0, 2, 1, 3).reshape(B, Sq, H * Dh) @ wo

    a = _A_ref(A_x, A_k_cache, A_v_cache, A_wq, A_wo)
    b = _B_ref(B_x, B_k_cache, B_v_cache, B_wq, B_wo)
    return torch.concatenate([a, b], axis=-1)
```

```cpp
#include <hip/hip_runtime.h>
#include <hip/hip_bf16.h>
#include <utility>
#include <stdint.h>

typedef float    f32x4  __attribute__((ext_vector_type(4)));
typedef float    f32x2  __attribute__((ext_vector_type(2)));
typedef __bf16   bf16x8 __attribute__((ext_vector_type(8)));
typedef __bf16   bf16x4 __attribute__((ext_vector_type(4)));
typedef __bf16   bf16x2 __attribute__((ext_vector_type(2)));
typedef unsigned u32x4  __attribute__((ext_vector_type(4)));
typedef unsigned u32x2  __attribute__((ext_vector_type(2)));

#define DI __device__ __forceinline__
#define LDSP __attribute__((address_space(3)))
#define FENCE() __builtin_amdgcn_sched_barrier(0)
#define AI __attribute__((always_inline))

template <class F, int... Is> DI void sfor_(F&& f, std::integer_sequence<int, Is...>) { (f(std::integral_constant<int, Is>{}), ...); }
template <int N, class F> DI void sfor(F&& f) { sfor_(f, std::make_integer_sequence<int, N>{}); }

constexpr int SQ = 160, DM = 448, NH = 16, SK = 1024, DH = 256;
constexpr unsigned SLOT = 32768u;
constexpr float SCL = 0.09016844005556021f;
constexpr float THR = 8.0f;
constexpr unsigned SRD3 = 0x00020000u;

DI unsigned rfl(unsigned x) { return __builtin_amdgcn_readfirstlane(x); }
DI u32x4 mk_srd(const void* p, unsigned bytes) {
  const uint64_t a = (uint64_t)p;
  u32x4 r; r[0] = rfl((unsigned)a); r[1] = rfl((unsigned)(a >> 32)); r[2] = bytes; r[3] = SRD3; return r;
}
DI unsigned cvtpk(float lo, float hi) { f32x2 v = {lo, hi}; bf16x2 b = __builtin_convertvector(v, bf16x2); return __builtin_bit_cast(unsigned, b); }
DI float max3f(float a, float b, float c) { float r; asm("v_max3_f32 %0, %1, %2, %3" : "=v"(r) : "v"(a), "v"(b), "v"(c)); return r; }
DI float max2f(float a, float b) { float r; asm("v_max_f32_e32 %0, %1, %2" : "=v"(r) : "v"(a), "v"(b)); return r; }
DI float fma_s(float a, float b, float c) { float r; asm("v_fma_f32 %0, %1, %2, %3" : "=v"(r) : "v"(a), "s"(b), "v"(c)); return r; }
DI void fma_ip(float& x, float s, float c) { asm volatile("v_fma_f32 %0, %0, %1, %2" : "+v"(x) : "s"(s), "v"(c)); }
DI void exp_ip(float& x) { asm volatile("v_exp_f32_e32 %0, %0" : "+v"(x)); }
DI unsigned cvt_v(float lo, float hi) { unsigned r; asm volatile("v_cvt_pk_bf16_f32 %0, %1, %2" : "=v"(r) : "v"(lo), "v"(hi)); return r; }
DI float acc_rd(float a) { float r; asm volatile("v_accvgpr_read_b32 %0, %1" : "=v"(r) : "a"(a)); return r; }
DI float acc_wr(float v) { float r; asm volatile("v_accvgpr_write_b32 %0, %1" : "=a"(r) : "v"(v)); return r; }
DI float acc_mul(float a, float f) { float r, t; asm volatile("v_accvgpr_read_b32 %1, %2\n\tv_mul_f32_e32 %1, %3, %1\n\tv_accvgpr_write_b32 %0, %1" : "=a"(r), "=&v"(t) : "0"(a), "v"(f)); return r; }
DI float mul_v(float a, float b) { float r; asm("v_mul_f32_e32 %0, %1, %2" : "=v"(r) : "v"(a), "v"(b)); return r; }

DI void mf_aa(f32x4& d, const bf16x8& a, const bf16x8& b) { asm volatile("v_mfma_f32_16x16x32_bf16 %0, %1, %2, %0" : "+a"(d) : "v"(a), "v"(b)); }
DI void mf_vv(f32x4& d, const bf16x8& a, const bf16x8& b) { asm volatile("v_mfma_f32_16x16x32_bf16 %0, %1, %2, %0" : "+v"(d) : "v"(a), "v"(b)); }
DI void mf_va(f32x4& d, const bf16x8& a, const bf16x8& b) { asm volatile("v_mfma_f32_16x16x32_bf16 %0, %1, %2, %0" : "+v"(d) : "v"(a), "a"(b)); }
DI void mf_vv0(f32x4& d, const bf16x8& a, const bf16x8& b) { asm volatile("v_mfma_f32_16x16x32_bf16 %0, %1, %2, 0" : "=&v"(d) : "v"(a), "v"(b)); }
DI void mf_va0(f32x4& d, const bf16x8& a, const bf16x8& b) { asm volatile("v_mfma_f32_16x16x32_bf16 %0, %1, %2, 0" : "=&v"(d) : "v"(a), "a"(b)); }

template <int MO, bool NT> DI unsigned dma(unsigned mb, unsigned so, unsigned step, unsigned voff, const u32x4& srd) {
  unsigned nso;
  if constexpr (NT) asm volatile("s_add_u32 m0, %1, %2\n\ts_add_u32 %0, %3, %4\n\tbuffer_load_dwordx4 %5, %6, %3 offen nt lds" : "=&s"(nso) : "s"(mb), "i"(MO), "s"(so), "s"(step), "v"(voff), "s"(srd) : "scc");
  else              asm volatile("s_add_u32 m0, %1, %2\n\ts_add_u32 %0, %3, %4\n\tbuffer_load_dwordx4 %5, %6, %3 offen lds"    : "=&s"(nso) : "s"(mb), "i"(MO), "s"(so), "s"(step), "v"(voff), "s"(srd) : "scc");
  return nso;
}
DI bf16x8 lds_b128(unsigned addr) { return *(const LDSP bf16x8*)addr; }
DI bf16x8 lds_tr2(unsigned addr0, unsigned addr1) {
  bf16x4 lo = __builtin_amdgcn_ds_read_tr16_b64_v4bf16((LDSP bf16x4*)addr0);
  bf16x4 hi = __builtin_amdgcn_ds_read_tr16_b64_v4bf16((LDSP bf16x4*)addr1);
  return __builtin_shufflevector(lo, hi, 0, 1, 2, 3, 4, 5, 6, 7);
}
DI void pf_load(unsigned& dummy, unsigned vo, const u32x4& srd) { asm volatile("buffer_load_dword %0, %1, %2, 0 offen" : "+v"(dummy) : "v"(vo), "s"(srd)); }
template <int OFF> DI void x_load(bf16x8& d, unsigned vo, const u32x4& srd) { asm volatile("buffer_load_dwordx4 %0, %1, %2, 0 offen offset:%3" : "=a"(d) : "v"(vo), "s"(srd), "i"(OFF)); }
DI void tie_a(bf16x8& d) { asm volatile("" : "+a"(d)); }
DI void tie_a4(f32x4& d) { asm volatile("" : "+a"(d)); }
DI void tie_v8(bf16x8& d) { asm volatile("" : "+v"(d)); }
DI void tie_v4(f32x4& d) { asm volatile("" : "+v"(d)); }
DI void tie_vf(float& d) { asm volatile("" : "+v"(d)); }
DI void tie_v(unsigned& d) { asm volatile("" : "+v"(d)); }
template <int N> DI void wait_vm() { asm volatile("s_waitcnt vmcnt(%0)" :: "i"(N) : "memory"); }
template <int N> DI void wait_bar() { asm volatile("s_waitcnt vmcnt(%0) lgkmcnt(0)\n\ts_barrier" :: "i"(N) : "memory"); }

struct Desc { u32x4 srd; unsigned so, step, v0, v1, mb; };

template <int NB> struct Ctx {
  f32x4  O[NB][16];
  f32x4  L[NB];
  bf16x8 Q[NB][8];
  f32x4  S[NB][4];
  u32x4  P[NB][2];
  float  nm[NB], dl[NB], mx[NB];
  bf16x8 ones;
  unsigned kbase, vbase;
  unsigned vK0, vK1, vV, vWo;
};


template <int I, bool NT> DI void dma_piece(Desc& d) { d.so = dma<4096 * I, NT>(d.mb, d.so, d.step, (I & 1) ? d.v1 : d.v0, d.srd); }

template <int NB, int KB0, int K> DI void sm_op(Ctx<NB>& c) {
  constexpr int NU = NB * 4;
  constexpr int U = K / 5, pos = K % 5;
  if constexpr (pos < 4) {
    if constexpr (U < NU) {
      constexpr int blk = U % NB, q = U / NB, kb = KB0 + (q >> 1), r = 2 * (q & 1) + (pos & 1);
      float x = c.S[blk][kb][r];
      if constexpr (pos < 2) fma_ip(x, SCL, c.nm[blk]); else exp_ip(x);
      c.S[blk][kb][r] = x;
    }
  }
}
template <int NB, int KB0, int U> DI void cv_op(Ctx<NB>& c) {
  constexpr int blk = U % NB, q = U / NB, kb = KB0 + (q >> 1), r = 2 * (q & 1);
  c.P[blk][KB0 >> 1][q] = cvt_v(c.S[blk][kb][r], c.S[blk][kb][r + 1]);
}

template <int NB, bool NT> DI void phaseA(Ctx<NB>& c, unsigned slot, Desc& d) {
  wait_bar<24>();
  const unsigned b0 = c.kbase + slot;
  unsigned kb4[4] = {b0, b0 ^ 64u, b0 ^ 128u, b0 ^ 192u};
  bf16x8 fr[4];
  auto rd = [&](auto F) AI { constexpr int f = F, kb = f >> 3, s = f & 7; fr[f & 3] = lds_b128(kb4[s & 3] + 256 * (s >> 2) + 8192 * kb); };
  rd(std::integral_constant<int, 0>{}); rd(std::integral_constant<int, 1>{}); rd(std::integral_constant<int, 2>{});
  constexpr int NU = NB * 4, NOPS = NU * 5 + 5;
  constexpr int GF = 22 * NB;
  FENCE();
  sfor<32>([&](auto F) AI {
    constexpr int f = F, kb = f >> 3, s = f & 7;
    sfor<NB>([&](auto Bk) AI {
      constexpr int blk = Bk, g = f * NB + blk;
      if constexpr (blk == 0) { if constexpr (s == 0) mf_va0(c.S[0][kb], fr[f & 3], c.Q[0][s]); else mf_va(c.S[0][kb], fr[f & 3], c.Q[0][s]); }
      else                    { if constexpr (s == 0) mf_vv0(c.S[blk][kb], fr[f & 3], c.Q[blk][s]); else mf_vv(c.S[blk][kb], fr[f & 3], c.Q[blk][s]); }
      if constexpr (blk == 0 && f + 3 < 32) rd(std::integral_constant<int, f + 3>{});
      if constexpr (blk == 1 && (f & 3) == 1) dma_piece<(f >> 2), NT>(d);
      constexpr int lo = (g < GF) ? (g * NOPS) / GF : NOPS, hi = (g + 1 < GF) ? ((g + 1) * NOPS) / GF : NOPS;
      sfor<hi - lo>([&](auto J) AI {
        constexpr int k = lo + J;
        if constexpr (k % 5 < 4) sm_op<NB, 2, k>(c);
        else if constexpr (k / 5 >= 1 && k / 5 - 1 < NU) cv_op<NB, 2, k / 5 - 1>(c);
      });
      FENCE();
    });
  });
}

template <int NB> DI float rowmax16(const f32x4* s) {
  float m = max3f(s[0][0], s[0][1], s[0][2]);
  m = max3f(m, s[0][3], s[1][0]); m = max3f(m, s[1][1], s[1][2]); m = max3f(m, s[1][3], s[2][0]);
  m = max3f(m, s[2][1], s[2][2]); m = max3f(m, s[2][3], s[3][0]); m = max3f(m, s[3][1], s[3][2]);
  return max2f(m, s[3][3]);
}
DI float xl32(float v) { auto p = __builtin_amdgcn_permlane32_swap(__float_as_uint(v), __float_as_uint(v), false, false); return max2f(__uint_as_float(p[0]), __uint_as_float(p[1])); }
DI float xl16(float v) { auto p = __builtin_amdgcn_permlane16_swap(__float_as_uint(v), __float_as_uint(v), false, false); return max2f(__uint_as_float(p[0]), __uint_as_float(p[1])); }

template <int NB> DI void phaseB0(Ctx<NB>& c) {
  asm volatile("s_nop 7" ::: "memory");
  FENCE();
  sfor<NB>([&](auto Bk) AI { constexpr int blk = Bk; c.mx[blk] = rowmax16<NB>(c.S[blk]); });
  sfor<NB>([&](auto Bk) AI { constexpr int blk = Bk; c.nm[blk] = -SCL * xl16(xl32(c.mx[blk])); });
  sfor<NB * 20 + 5>([&](auto J) AI { constexpr int k = J;
    if constexpr (k % 5 < 4) sm_op<NB, 0, k>(c);
    else if constexpr (k / 5 >= 1 && k / 5 - 1 < NB * 4) cv_op<NB, 0, k / 5 - 1>(c); });
  sfor<NB>([&](auto Bk) AI { constexpr int blk = Bk;
    c.L[blk] = f32x4{0.f, 0.f, 0.f, 0.f};
    sfor<16>([&](auto Db) AI { c.O[blk][Db] = f32x4{0.f, 0.f, 0.f, 0.f}; tie_a4(c.O[blk][Db]); }); });
}

template <int NB, bool FINAL, bool NT> DI void phaseB(Ctx<NB>& c, unsigned slot, Desc& d) {
  wait_bar<24>();
  const unsigned b0 = c.vbase + slot;
  bf16x8 fr[4];
  unsigned vb;
  unsigned vbj[2];
  auto rd = [&](auto F) AI {
    constexpr int f = F, u = f >> 4, j = (f >> 1) & 7, hi = f & 1;
    if constexpr (hi == 0) vbj[j & 1] = b0 ^ (32u * j);
    fr[f & 3] = lds_tr2(vbj[j & 1] + 256 * hi + 16384 * u, vbj[j & 1] + 256 * hi + 16384 * u + 8192);
  };
  rd(std::integral_constant<int, 0>{}); rd(std::integral_constant<int, 1>{}); rd(std::integral_constant<int, 2>{});
  unsigned long long chg = 0;
  if constexpr (!FINAL) {
    asm volatile("s_nop 1" ::: "memory");
    sfor<NB>([&](auto Bk) AI { constexpr int blk = Bk; c.mx[blk] = rowmax16<NB>(c.S[blk]); });
  }
  FENCE();
  constexpr int NU = NB * 4;
  constexpr int GH = 17 * NB;
  sfor<2>([&](auto U_) AI {
    constexpr int u = U_;
    sfor<17>([&](auto FF) AI {
      constexpr int ff = FF;
      constexpr int f = 16 * u + ff - 1;
      sfor<NB>([&](auto Bk) AI {
        constexpr int blk = Bk, g = u * GH + ff * NB + blk;
        if constexpr (ff == 0) mf_vv(c.L[blk], c.ones, __builtin_bit_cast(bf16x8, c.P[blk][u]));
        else {
          constexpr int db = ((f >> 1) & 7) + 8 * (f & 1);
          mf_aa(c.O[blk][db], fr[f & 3], __builtin_bit_cast(bf16x8, c.P[blk][u]));
          if constexpr (blk == 0 && f + 3 < 32) rd(std::integral_constant<int, f + 3>{});
          if constexpr (blk == 1 && (f & 3) == 1) dma_piece<(f >> 2), NT>(d);
        }
        if constexpr (FINAL) {
          constexpr int NOPS = NU * 5 + 5, GF = GH - 2;
          constexpr int lo = (g < GF) ? (g * NOPS) / GF : NOPS, hi = (g + 1 < GF) ? ((g + 1) * NOPS) / GF : NOPS;
          sfor<hi - lo>([&](auto J) AI { constexpr int k = lo + J;
            if constexpr (k % 5 < 4) sm_op<NB, 2, k>(c);
            else if constexpr (k / 5 >= 1 && k / 5 - 1 < NU) cv_op<NB, 2, k / 5 - 1>(c); });
        } else {
          if constexpr (g < NB) c.mx[g] = xl32(c.mx[g]);
          else if constexpr (g < 2 * NB) c.mx[g - NB] = xl16(c.mx[g - NB]);
          else if constexpr (g < 3 * NB) {
            constexpr int b = g - 2 * NB;
            const float t = fma_s(c.mx[b], SCL, c.nm[b]);
            const bool cond = t > THR;
            c.dl[b] = cond ? t : 0.f;
            c.nm[b] -= c.dl[b];
            chg |= __ballot(cond);
          } else {
            constexpr int G0 = 3 * NB + 1, G1 = 2 * GH - NB - 1, NOPS = NU * 4;
            constexpr int lo = (g < G0) ? 0 : ((g - G0 < G1 - G0) ? ((g - G0) * NOPS) / (G1 - G0) : NOPS);
            constexpr int hi = (g + 1 < G0) ? 0 : ((g + 1 - G0 < G1 - G0) ? ((g + 1 - G0) * NOPS) / (G1 - G0) : NOPS);
            sfor<hi - lo>([&](auto J) AI { constexpr int k = lo + J; sm_op<NB, 0, (k / 4) * 5 + (k % 4)>(c); });
            sfor<NU>([&](auto UU) AI {
              constexpr int un = UU;
              constexpr int gdone = G0 + ((4 * un + 4) * (G1 - G0) + NOPS - 1) / NOPS + 1;
              constexpr int gmin = GH + 1 + un;
              constexpr int gat = gdone > gmin ? gdone : gmin;
              if constexpr (g == (gat < 2 * GH - 1 ? gat : 2 * GH - 1)) cv_op<NB, 0, un>(c);
            });
          }
        }
        FENCE();
      });
    });
  });
  if constexpr (!FINAL) {
    if (__builtin_expect(chg != 0ull, 0)) {
      asm volatile("s_nop 7" ::: "memory");
      FENCE();
      sfor<NB>([&](auto Bk) AI { constexpr int blk = Bk;
        const float f = __builtin_amdgcn_exp2f(-c.dl[blk]);
        c.L[blk] *= f;
        sfor<16>([&](auto Db) AI { f32x4 o = c.O[blk][Db];
          o[0] = acc_mul(o[0], f); o[1] = acc_mul(o[1], f); o[2] = acc_mul(o[2], f); o[3] = acc_mul(o[3], f);
          c.O[blk][Db] = o; }); });
      FENCE();
      asm volatile("s_nop 3" ::: "memory");
    }
  }
}

template <int NB>
DI void body(const __hip_bfloat16* __restrict__ x, const __hip_bfloat16* __restrict__ kc, const __hip_bfloat16* __restrict__ vc,
             const __hip_bfloat16* __restrict__ wq, const __hip_bfloat16* __restrict__ wo, char* __restrict__ part,
             int b, int h, int grp, unsigned w, unsigned lane, unsigned lds0) {
  const unsigned g = lane >> 4, cc = lane & 15u, q4 = cc >> 2, p4 = lane & 3u, hi5 = lane >> 5, ch = lane & 31u;
  Ctx<NB> c;
  const u32x4 srdK = mk_srd((const char*)kc + ((size_t)(b * NH + h) * SK) * (DH * 2), SK * DH * 2);
  const u32x4 srdV = mk_srd((const char*)vc + ((size_t)(b * NH + h) * SK) * (DH * 2), SK * DH * 2);
  const u32x4 srdWq = mk_srd((const char*)wq + h * (DH * 2), DM * NH * DH * 2 - h * (DH * 2));
  const u32x4 srdWo = mk_srd((const char*)wo + (size_t)h * DH * DM * 2, DH * DM * 2);
  const u32x4 srdX = mk_srd((const char*)x + (size_t)b * SQ * DM * 2, SQ * DM * 2);
  const unsigned rb[3] = {2u * w, 2u * w + 1u, 8u + w};

  bf16x8 xf[14][NB];
  unsigned dummy = 0;
  const unsigned vWq = 8192u * hi5 + 16u * (ch ^ (4u * (2u * (w & 1u) + hi5)));
  const unsigned mbw = rfl(lds0 + 1024u * (w & 1u));
  if constexpr (NB == 3) {
    if (w == 0) {
      sfor<4>([&](auto I) AI {
        unsigned idx = 64u * I + lane; idx = idx < 224u ? idx : 223u;
        const unsigned vo = (56u * b + (idx >> 2)) * 8192u + (idx & 3u) * 128u;
        pf_load(dummy, vo, srdWq);
      });
    }
  } else {
    unsigned so = rfl(2u * (w & 1u) * 8192u);
    sfor<16>([&](auto I) AI { so = dma<SLOT * 1 + 2048 * I, false>(mbw, so, 4u * 8192u, vWq, srdWq); });
  }
  {
    unsigned vx[NB];
    sfor<NB>([&](auto Bk) AI { vx[Bk] = (16u * rb[Bk] + cc) * (DM * 2) + 16u * g; });
    sfor<14>([&](auto Ks) AI { sfor<NB>([&](auto Bk) AI {
      x_load<64 * (int)Ks>(xf[Ks][Bk], vx[Bk], srdX); }); });
  }
  if constexpr (NB == 3) {
    sfor<2>([&](auto T) AI {
      unsigned so = rfl(T * SLOT + 1024u * w);
      sfor<16>([&](auto I) AI {
        const unsigned vo = 512u * hi5 + 16u * (ch ^ hi5 ^ ((4u * I + 2u * w) & 15u));
        so = dma<SLOT * (2 + T) + 2048 * I, true>(mbw, so, 2048u, vo, srdK); });
    });
    unsigned so = rfl(1024u * w);
    sfor<16>([&](auto I) AI {
      const unsigned vo = 512u * hi5 + 16u * (ch ^ (2u * ((2u * (2u * (I & 1) + w) + hi5) & 7u)));
      so = dma<SLOT * 4 + 2048 * I, true>(mbw, so, 2048u, vo, srdV); });
  } else {
    unsigned so = rfl((64u + 2u * (w & 1u)) * 8192u);
    sfor<16>([&](auto I) AI { so = dma<SLOT * 0 + 2048 * I, false>(mbw, so, 4u * 8192u, vWq, srdWq); });
  }

  c.kbase = lds0 + 512u * cc + 16u * (g ^ (cc & 3u)) + 64u * (cc >> 2);
  c.vbase = lds0 + 512u * (4u * g + q4) + 32u * (4u * (g & 1u) + q4) + 8u * p4;
  c.vK0 = 512u * hi5 + 16u * (ch ^ hi5 ^ (2u * w));
  c.vK1 = 512u * hi5 + 16u * (ch ^ hi5 ^ (2u * w + 8u));
  c.vV  = 512u * hi5 + 16u * (ch ^ (2u * (2u * w + hi5)));
  c.vWo = 896u * (lane >> 3) + 16u * ((lane & 7u) ^ (2u * ((lane >> 4) & 3u)));
  { u32x4 o = {0x3F803F80u, 0x3F803F80u, 0x3F803F80u, 0x3F803F80u}; c.ones = __builtin_bit_cast(bf16x8, o); tie_v8(c.ones); }
  const unsigned mb4 = rfl(lds0 + 1024u * w);

  auto desc = [&](int n) AI {
    Desc d; d.mb = mb4 + (unsigned)((n + 2) % 5) * SLOT;
    if (n >= 32)      { d.srd = srdWo; d.so = 7168u * w + 128u * (unsigned)(n - 32); d.step = 4u * 7168u; d.v0 = d.v1 = c.vWo; }
    else if ((n & 1) && n != 31) { d.srd = srdK; d.so = (unsigned)((n + 1) >> 1) * SLOT + 1024u * w; d.step = 4096u; d.v0 = c.vK0; d.v1 = c.vK1; }
    else              { d.srd = srdV; d.so = (unsigned)(n == 31 ? 15 : ((n - 2) >> 1)) * SLOT + 1024u * w; d.step = 4096u; d.v0 = d.v1 = c.vV; }
    return d;
  };

  {
    f32x4 qa[NB][16];
    const unsigned wbase = lds0 + 512u * (8u * g + q4) + 64u * q4 + 16u * p4;
    sfor<7>([&](auto C_) AI {
      constexpr int C = C_;
      if constexpr (NB == 3) { if constexpr (C == 0) asm volatile("s_waitcnt vmcnt(48)" ::: "memory"); }
      else { if constexpr (C == 0) asm volatile("s_waitcnt vmcnt(16)" ::: "memory"); else asm volatile("s_waitcnt vmcnt(0)" ::: "memory"); }
      if constexpr (C == 0) {
        tie_v(dummy);
        sfor<14>([&](auto Ks) AI { sfor<NB>([&](auto Bk) AI { tie_a(xf[Ks][Bk]); }); });
      }
      asm volatile("s_waitcnt lgkmcnt(0)\n\ts_barrier" ::: "memory");
      const unsigned b0 = wbase + SLOT * ((C + 1) & 1);
      const unsigned wb4[4] = {b0, b0 ^ 64u, b0 ^ 128u, b0 ^ 192u};
      bf16x8 fr[4];
      auto rd = [&](auto F) AI { constexpr int f = F, sp = f >> 2, half = (f >> 1) & 1, u = f & 1;
        const unsigned a = wb4[sp & 3] + 256 * (sp >> 2) + 8 * half + 16384 * u; fr[f & 3] = lds_tr2(a, a + 2048); };
      rd(std::integral_constant<int, 0>{}); rd(std::integral_constant<int, 1>{}); rd(std::integral_constant<int, 2>{});
      unsigned so = 0; Desc d3;
      if constexpr (NB == 2 && C >= 1 && C <= 5) so = rfl((64u * (C + 1) + 2u * (w & 1u)) * 8192u);
      if constexpr (C == 6) d3 = desc(3);
      FENCE();
      sfor<32>([&](auto F) AI {
        constexpr int f = F, sp = f >> 2, half = (f >> 1) & 1, u = f & 1, be = 2 * sp + half;
        sfor<NB>([&](auto Bk) AI {
          constexpr int blk = Bk;
          if constexpr (C == 0 && u == 0) mf_va0(qa[blk][be], fr[f & 3], xf[2 * C + u][blk]); else mf_va(qa[blk][be], fr[f & 3], xf[2 * C + u][blk]);
          if constexpr (blk == 0 && f + 3 < 32) rd(std::integral_constant<int, f + 3>{});
          if constexpr (blk == 1) {
            if constexpr (NB == 2 && C >= 1 && C <= 5 && (f & 1) == 0) so = dma<SLOT * (C & 1) + 2048 * (f >> 1), false>(mbw, so, 4u * 8192u, vWq, srdWq);
            if constexpr (C == 6 && (f & 3) == 1) dma_piece<(f >> 2), true>(d3);
          }
          FENCE();
        });
      });
    });
    asm volatile("s_nop 7" ::: "memory");
    FENCE();
    sfor<NB>([&](auto Bk) AI { sfor<8>([&](auto Sp) AI {
      constexpr int blk = Bk, sp = Sp;
      u32x4 t = {cvtpk(qa[blk][2 * sp][0], qa[blk][2 * sp][1]), cvtpk(qa[blk][2 * sp][2], qa[blk][2 * sp][3]),
                 cvtpk(qa[blk][2 * sp + 1][0], qa[blk][2 * sp + 1][1]), cvtpk(qa[blk][2 * sp + 1][2], qa[blk][2 * sp + 1][3])};
      c.Q[blk][sp] = __builtin_bit_cast(bf16x8, t);
      if constexpr (blk == 0) tie_a(c.Q[blk][sp]);
      FENCE(); }); });
    sfor<NB>([&](auto Bk) AI { c.nm[Bk] = 0.f; sfor<4>([&](auto Kb) AI { c.S[Bk][Kb] = f32x4{0.f, 0.f, 0.f, 0.f}; }); });
    FENCE();
    asm volatile("s_nop 1" ::: "memory");
  }

  {
    Desc d = desc(4);
    phaseA<NB, true>(c, 2u * SLOT, d);
    phaseB0<NB>(c);
  }
  unsigned sl = 3;
  for (int t = 1; t < 16; ++t) {
    { Desc d = desc(2 * t + 3); phaseA<NB, true>(c, sl * SLOT, d); }
    sl = sl == 4 ? 0 : sl + 1;
    { Desc d = desc(2 * t + 4); phaseB<NB, false, true>(c, sl * SLOT, d); }
    sl = sl == 4 ? 0 : sl + 1;
  }
  { Desc d = desc(35); phaseB<NB, true, true>(c, sl * SLOT, d); }
  sl = sl == 4 ? 0 : sl + 1;

  asm volatile("s_nop 7" ::: "memory");
  FENCE();
  bf16x8 of[NB][8];
  float rl[NB];
  sfor<NB>([&](auto Bk) AI { constexpr int blk = Bk;
    tie_v4(c.L[blk]);
    rl[blk] = __builtin_amdgcn_rcpf(c.L[blk][0]);
    sfor<8>([&](auto Sp) AI { constexpr int sp = Sp;
      const f32x4 o0 = c.O[blk][2 * sp], o1 = c.O[blk][2 * sp + 1];
      const float a0 = acc_rd(o0[0]), a1 = acc_rd(o0[1]), a2 = acc_rd(o0[2]), a3 = acc_rd(o0[3]);
      const float a4 = acc_rd(o1[0]), a5 = acc_rd(o1[1]), a6 = acc_rd(o1[2]), a7 = acc_rd(o1[3]);
      u32x4 t = {cvt_v(a0, a1), cvt_v(a2, a3), cvt_v(a4, a5), cvt_v(a6, a7)};
      of[blk][sp] = __builtin_bit_cast(bf16x8, t); }); });
  FENCE();
  asm volatile("s_nop 1" ::: "memory");
  const unsigned obase = lds0 + 128u * (4u * g + q4) + 32u * ((2u * g + (q4 >> 1)) & 3u) + 8u * p4;
  const __amdgpu_buffer_rsrc_t rsP = __builtin_amdgcn_make_buffer_rsrc((void*)(part + (size_t)(grp * NH + h) * (140 * 1024)), (short)0, 140 * 1024, SRD3);
  unsigned vP[NB];
  sfor<NB>([&](auto Bk) AI { vP[Bk] = rb[Bk] * (14u * 1024u) + lane * 16u; });
  for (int cw = 0; cw < 7; ++cw) {
    if (cw < 4) { if constexpr (NB == 3) asm volatile("s_waitcnt vmcnt(24)" ::: "memory"); else asm volatile("s_waitcnt vmcnt(24)" ::: "memory"); }
    else asm volatile("s_waitcnt vmcnt(0)" ::: "memory");
    asm volatile("s_waitcnt lgkmcnt(0)\n\ts_barrier" ::: "memory");
    const unsigned b0 = obase + sl * SLOT;
    sl = sl == 4 ? 0 : sl + 1;
    if (cw < 3) { Desc d = desc(36 + cw); sfor<8>([&](auto I) AI { dma_piece<I, false>(d); }); }
    const unsigned ob4[4] = {b0, b0 ^ 32u, b0 ^ 64u, b0 ^ 96u};
    bf16x8 fr[4];
    f32x4 y[NB][4];
    auto rd = [&](auto F) AI { constexpr int f = F, nb = f >> 3, s = f & 7; const unsigned a = ob4[nb & 3] + 4096 * s; fr[f & 3] = lds_tr2(a, a + 2048); };
    rd(std::integral_constant<int, 0>{}); rd(std::integral_constant<int, 1>{}); rd(std::integral_constant<int, 2>{});
    FENCE();
    sfor<32>([&](auto F) AI {
      constexpr int f = F, nb = f >> 3, s = f & 7;
      sfor<NB>([&](auto Bk) AI {
        constexpr int blk = Bk;
        if constexpr (s == 0) mf_vv0(y[blk][nb], fr[f & 3], of[blk][s]); else mf_vv(y[blk][nb], fr[f & 3], of[blk][s]);
        if constexpr (blk == 0 && f + 3 < 32) rd(std::integral_constant<int, f + 3>{});
        FENCE();
      });
    });
    asm volatile("s_nop 7" ::: "memory");
    FENCE();
    sfor<NB>([&](auto Bk) AI { sfor<2>([&](auto I) AI {
      constexpr int blk = Bk, i = I;
      const float r = rl[blk];
      u32x4 t = {cvtpk(mul_v(y[blk][2 * i][0], r), mul_v(y[blk][2 * i][1], r)), cvtpk(mul_v(y[blk][2 * i][2], r), mul_v(y[blk][2 * i][3], r)),
                 cvtpk(mul_v(y[blk][2 * i + 1][0], r), mul_v(y[blk][2 * i + 1][1], r)), cvtpk(mul_v(y[blk][2 * i + 1][2], r), mul_v(y[blk][2 * i + 1][3], r))};
      __builtin_amdgcn_raw_buffer_store_b128(t, rsP, vP[blk], (2u * cw + i) * 1024u, 0); }); });
    FENCE();
  }
}

__global__ __launch_bounds__(256, 1)
void attn_kernel(const __hip_bfloat16* __restrict__ xA, const __hip_bfloat16* __restrict__ kA, const __hip_bfloat16* __restrict__ vA,
                 const __hip_bfloat16* __restrict__ wqA, const __hip_bfloat16* __restrict__ woA,
                 const __hip_bfloat16* __restrict__ xB, const __hip_bfloat16* __restrict__ kB, const __hip_bfloat16* __restrict__ vB,
                 const __hip_bfloat16* __restrict__ wqB, const __hip_bfloat16* __restrict__ woB, char* __restrict__ part) {
  extern __shared__ __attribute__((aligned(16))) char lds[];
  const unsigned blk = blockIdx.x, xcd = blk & 7u, j = blk >> 3;
  const int br = xcd & 1u, h = (xcd >> 1) * 4 + (j & 3u), b = j >> 2;
  const unsigned lane = threadIdx.x & 63u, w = rfl(threadIdx.x >> 6);
  const unsigned lds0 = (unsigned)(size_t)lds;
  const __hip_bfloat16* x  = br ? xB : xA;
  const __hip_bfloat16* kc = br ? kB : kA;
  const __hip_bfloat16* vc = br ? vB : vA;
  const __hip_bfloat16* wq = br ? wqB : wqA;
  const __hip_bfloat16* wo = br ? woB : woA;
  if (w < 2) body<3>(x, kc, vc, wq, wo, part, b, h, br * 8 + b, w, lane, lds0);
  else       body<2>(x, kc, vc, wq, wo, part, b, h, br * 8 + b, w, lane, lds0);
}

__global__ __launch_bounds__(256)
void reduce_kernel(const char* __restrict__ part, __hip_bfloat16* __restrict__ out) {
  const unsigned id = blockIdx.x * 256u + threadIdx.x;
  const unsigned lane = id & 63u, fr = (id >> 6) % 140u, grp = (id >> 6) / 140u;
  const char* p = part + ((size_t)grp * 16 * 140 + fr) * 1024 + lane * 16;
  float s[8] = {0, 0, 0, 0, 0, 0, 0, 0};
  u32x4 v[16];
#pragma unroll
  for (int h = 0; h < 16; ++h) v[h] = *(const u32x4*)(p + (size_t)h * 140 * 1024);
#pragma unroll
  for (int h = 0; h < 16; ++h)
#pragma unroll
    for (int i = 0; i < 4; ++i) { s[2 * i] += __uint_as_float(v[h][i] << 16); s[2 * i + 1] += __uint_as_float(v[h][i] & 0xffff0000u); }
  const unsigned rb = fr / 14u, pr = fr % 14u, g = lane >> 4, cc = lane & 15u;
  const unsigned br = grp >> 3, b = grp & 7u;
  char* o = (char*)out + ((size_t)(b * SQ + 16u * rb + cc) * (2 * DM) + br * DM + 32u * pr + 4u * g) * 2;
  u32x2 a = {cvtpk(s[0], s[1]), cvtpk(s[2], s[3])}, d = {cvtpk(s[4], s[5]), cvtpk(s[6], s[7])};
  *(u32x2*)o = a;
  *(u32x2*)(o + 32) = d;
}

extern "C" void kernel_launch(void* const* d_in, const int* in_sizes, int n_in, void* d_out, int out_size, void* d_ws, size_t ws_size,
                              hipStream_t stream) {
  (void)in_sizes; (void)n_in; (void)out_size; (void)ws_size;
  typedef const __hip_bfloat16* P;
  (void)hipFuncSetAttribute((const void*)attn_kernel, hipFuncAttributeMaxDynamicSharedMemorySize, 163840);
  attn_kernel<<<256, 256, 163840, stream>>>((P)d_in[0], (P)d_in[1], (P)d_in[2], (P)d_in[3], (P)d_in[4],
                                            (P)d_in[5], (P)d_in[6], (P)d_in[7], (P)d_in[8], (P)d_in[9], (char*)d_ws);
  reduce_kernel<<<560, 256, 0, stream>>>((const char*)d_ws, (__hip_bfloat16*)d_out);
}
```

```cpp
#include <hip/hip_runtime.h>
#include <hip/hip_bf16.h>
#include <utility>
#include <stdint.h>

typedef float    f32x4  __attribute__((ext_vector_type(4)));
typedef float    f32x2  __attribute__((ext_vector_type(2)));
typedef __bf16   bf16x8 __attribute__((ext_vector_type(8)));
typedef __bf16   bf16x4 __attribute__((ext_vector_type(4)));
typedef __bf16   bf16x2 __attribute__((ext_vector_type(2)));
typedef unsigned u32x4  __attribute__((ext_vector_type(4)));
typedef unsigned u32x2  __attribute__((ext_vector_type(2)));

#define DI __device__ __forceinline__
#define LDSP __attribute__((address_space(3)))
#define FENCE() __builtin_amdgcn_sched_barrier(0)
#define AI __attribute__((always_inline))

template <class F, int... Is> DI void sfor_(F&& f, std::integer_sequence<int, Is...>) { (f(std::integral_constant<int, Is>{}), ...); }
template <int N, class F> DI void sfor(F&& f) { sfor_(f, std::make_integer_sequence<int, N>{}); }
template <int V> using IC = std::integral_constant<int, V>;

constexpr int SQ = 160, DM = 448, NH = 16, SK = 1024, DH = 256;
constexpr unsigned SLOT = 32768u;
constexpr float SCL = 0.09016844005556021f;
constexpr float THR = 8.0f;
constexpr unsigned SRD3 = 0x00020000u;
constexpr unsigned PARTB = 140u * 1024u;
constexpr size_t FLAG_OFF = 40u << 20;
constexpr unsigned OOB = 0x40000000u;
constexpr unsigned CODE_PF = 64u * 1024u;

DI unsigned lane_id() { unsigned l; asm volatile("v_mbcnt_lo_u32_b32 %0, -1, 0\n\tv_mbcnt_hi_u32_b32 %0, -1, %0" : "=v"(l)); return l; }
DI unsigned rfl(unsigned x) { return __builtin_amdgcn_readfirstlane(x); }
DI u32x4 mk_srd(const void* p, unsigned bytes) {
  const uint64_t a = (uint64_t)p;
  u32x4 r; r[0] = rfl((unsigned)a); r[1] = rfl((unsigned)(a >> 32)); r[2] = bytes; r[3] = SRD3; return r;
}
DI unsigned cvtpk(float lo, float hi) { f32x2 v = {lo, hi}; bf16x2 b = __builtin_convertvector(v, bf16x2); return __builtin_bit_cast(unsigned, b); }
DI float max3f(float a, float b, float c) { float r; asm("v_max3_f32 %0, %1, %2, %3" : "=v"(r) : "v"(a), "v"(b), "v"(c)); return r; }
DI float max2f(float a, float b) { float r; asm("v_max_f32_e32 %0, %1, %2" : "=v"(r) : "v"(a), "v"(b)); return r; }
DI float fma_s(float a, float b, float c) { float r; asm("v_fma_f32 %0, %1, %2, %3" : "=v"(r) : "v"(a), "s"(b), "v"(c)); return r; }
DI void fma_ip(float& x, float s, float c) { asm volatile("v_fma_f32 %0, %0, %1, %2" : "+v"(x) : "s"(s), "v"(c)); }
DI void exp_ip(float& x) { asm volatile("v_exp_f32_e32 %0, %0" : "+v"(x)); }
DI unsigned cvt_v(float lo, float hi) { unsigned r; asm volatile("v_cvt_pk_bf16_f32 %0, %1, %2" : "=v"(r) : "v"(lo), "v"(hi)); return r; }
DI float acc_rd(float a) { float r; asm volatile("v_accvgpr_read_b32 %0, %1" : "=v"(r) : "a"(a)); return r; }
DI float acc_mul(float a, float f) { float r, t; asm volatile("v_accvgpr_read_b32 %1, %2\n\tv_mul_f32_e32 %1, %3, %1\n\tv_accvgpr_write_b32 %0, %1" : "=a"(r), "=&v"(t) : "0"(a), "v"(f)); return r; }
DI float mul_v(float a, float b) { float r; asm volatile("v_mul_f32_e32 %0, %1, %2" : "=v"(r) : "v"(a), "v"(b)); return r; }

DI void mf_aa(f32x4& d, const bf16x8& a, const bf16x8& b) { asm volatile("v_mfma_f32_16x16x32_bf16 %0, %1, %2, %0" : "+a"(d) : "v"(a), "v"(b)); }
DI void mf_vv(f32x4& d, const bf16x8& a, const bf16x8& b) { asm volatile("v_mfma_f32_16x16x32_bf16 %0, %1, %2, %0" : "+v"(d) : "v"(a), "v"(b)); }
DI void mf_va(f32x4& d, const bf16x8& a, const bf16x8& b) { asm volatile("v_mfma_f32_16x16x32_bf16 %0, %1, %2, %0" : "+v"(d) : "v"(a), "a"(b)); }
DI void mf_vv0(f32x4& d, const bf16x8& a, const bf16x8& b) { asm volatile("v_mfma_f32_16x16x32_bf16 %0, %1, %2, 0" : "=&v"(d) : "v"(a), "v"(b)); }
DI void mf_va0(f32x4& d, const bf16x8& a, const bf16x8& b) { asm volatile("v_mfma_f32_16x16x32_bf16 %0, %1, %2, 0" : "=&v"(d) : "v"(a), "a"(b)); }

template <int MO, bool NT> DI unsigned dma(unsigned mb, unsigned so, unsigned step, unsigned voff, const u32x4& srd) {
  unsigned nso;
  if constexpr (NT) asm volatile("s_add_u32 m0, %1, %2\n\ts_add_u32 %0, %3, %4\n\tbuffer_load_dwordx4 %5, %6, %3 offen nt lds" : "=&s"(nso) : "s"(mb), "i"(MO), "s"(so), "s"(step), "v"(voff), "s"(srd) : "scc");
  else              asm volatile("s_add_u32 m0, %1, %2\n\ts_add_u32 %0, %3, %4\n\tbuffer_load_dwordx4 %5, %6, %3 offen lds"    : "=&s"(nso) : "s"(mb), "i"(MO), "s"(so), "s"(step), "v"(voff), "s"(srd) : "scc");
  return nso;
}
DI bf16x8 lds_b128(unsigned addr) { return *(const LDSP bf16x8*)addr; }
DI bf16x8 lds_tr2(unsigned addr0, unsigned addr1) {
  bf16x4 lo = __builtin_amdgcn_ds_read_tr16_b64_v4bf16((LDSP bf16x4*)addr0);
  bf16x4 hi = __builtin_amdgcn_ds_read_tr16_b64_v4bf16((LDSP bf16x4*)addr1);
  return __builtin_shufflevector(lo, hi, 0, 1, 2, 3, 4, 5, 6, 7);
}
DI void pf_load(unsigned& dummy, unsigned vo, const u32x4& srd) { asm volatile("buffer_load_dword %0, %1, %2, 0 offen" : "+v"(dummy) : "v"(vo), "s"(srd)); }
template <int OFF> DI void x_load(bf16x8& d, unsigned vo, const u32x4& srd) { asm volatile("buffer_load_dwordx4 %0, %1, %2, 0 offen offset:%3" : "=a"(d) : "v"(vo), "s"(srd), "i"(OFF)); }
DI void ld8_sc1(u32x2& d, unsigned vo, const u32x4& srd) { asm volatile("buffer_load_dwordx2 %0, %1, %2, 0 offen sc1" : "=v"(d) : "v"(vo), "s"(srd)); }
DI void st8_sc1(const u32x2& d, unsigned vo, unsigned so, const u32x4& srd) { asm volatile("buffer_store_dwordx2 %0, %1, %2, %3 offen sc1" :: "v"(d), "v"(vo), "s"(srd), "s"(so) : "memory"); }
DI void st16_sc1(const u32x4& d, unsigned vo, unsigned so, const u32x4& srd) { asm volatile("buffer_store_dwordx4 %0, %1, %2, %3 offen sc1" :: "v"(d), "v"(vo), "s"(srd), "s"(so) : "memory"); }
DI void st16(const u32x4& d, unsigned vo, unsigned so, const u32x4& srd) { asm volatile("buffer_store_dwordx4 %0, %1, %2, %3 offen" :: "v"(d), "v"(vo), "s"(srd), "s"(so) : "memory"); }
template <int OFF> DI void ld16a_sc1(bf16x8& d, unsigned vo, unsigned so, const u32x4& srd) { asm volatile("buffer_load_dwordx4 %0, %1, %2, %3 offen offset:%4 sc1" : "=a"(d) : "v"(vo), "s"(srd), "s"(so), "i"(OFF)); }
#define XCLOB "a88","a89","a90","a91","a92","a93","a94","a95","a96","a97","a98","a99","a100","a101","a102","a103","a104","a105","a106","a107","a108","a109","a110","a111", \
  "a112","a113","a114","a115","a116","a117","a118","a119","a120","a121","a122","a123","a124","a125","a126","a127","a128","a129","a130","a131","a132","a133","a134","a135", \
  "a136","a137","a138","a139","a140","a141","a142","a143","a144","a145","a146","a147","a148","a149","a150","a151","a152","a153","a154","a155","a156","a157","a158","a159", \
  "a160","a161","a162","a163","a164","a165","a166","a167","a168","a169","a170","a171","a172","a173","a174","a175","a176","a177","a178","a179","a180","a181","a182","a183", \
  "a184","a185","a186","a187","a188","a189","a190","a191","a192","a193","a194","a195","a196","a197","a198","a199","a200","a201","a202","a203","a204","a205","a206","a207", \
  "a208","a209","a210","a211","a212","a213","a214","a215","a216","a217","a218","a219","a220","a221","a222","a223","a224","a225","a226","a227","a228","a229","a230","a231", \
  "a232","a233","a234","a235","a236","a237","a238","a239","a240","a241","a242","a243","a244","a245","a246","a247","a248","a249","a250","a251","a252","a253","a254","a255"
template <int R, int OFF> DI void xa_load(unsigned vo, const u32x4& srd) {
  asm volatile("buffer_load_dwordx4 a[%c0:%c1], %2, %3, 0 offen offset:%4" :: "i"(R), "i"(R + 3), "v"(vo), "s"(srd), "i"(OFF) : XCLOB); }
template <int R> DI void xa_perm(unsigned pa) { asm volatile("ds_bpermute_b32 a%c0, %1, a%c0" :: "i"(R), "v"(pa) : XCLOB); }
template <int R> DI void mf_vx(f32x4& d, const bf16x8& a) { asm volatile("v_mfma_f32_16x16x32_bf16 %0, %1, a[%c2:%c3], %0" : "+v"(d) : "v"(a), "i"(R), "i"(R + 3)); }
template <int R> DI void mf_vx0(f32x4& d, const bf16x8& a) { asm volatile("v_mfma_f32_16x16x32_bf16 %0, %1, a[%c2:%c3], 0" : "=&v"(d) : "v"(a), "i"(R), "i"(R + 3)); }
DI void swap32(unsigned& a, unsigned& b) { asm volatile("v_permlane32_swap_b32 %0, %1" : "+v"(a), "+v"(b)); }
DI void swap16(unsigned& a, unsigned& b) { asm volatile("v_permlane16_swap_b32 %0, %1" : "+v"(a), "+v"(b)); }
DI void tie_a(bf16x8& d) { asm volatile("" : "+a"(d)); }
DI void tie_a4(f32x4& d) { asm volatile("" : "+a"(d)); }
DI void tie_v8(bf16x8& d) { asm volatile("" : "+v"(d)); }
DI void tie_v4(f32x4& d) { asm volatile("" : "+v"(d)); }
DI void tie_v2(u32x2& d) { asm volatile("" : "+v"(d)); }
DI void tie_v(unsigned& d) { asm volatile("" : "+v"(d)); }
template <int N> DI void wait_vm() { asm volatile("s_waitcnt vmcnt(%0)" :: "i"(N) : "memory"); }
template <int N> DI void wait_bar() { asm volatile("s_waitcnt vmcnt(%0) lgkmcnt(0)\n\ts_barrier" :: "i"(N) : "memory"); }

struct Prof {};
#define PRE(i)
#define POST(i)
#define RT(i)

struct Desc { u32x4 srd; unsigned so, step, v0, v1, mb; };

template <int NB> struct Ctx {
  f32x4  O[NB][16];
  f32x4  L[NB];
  bf16x8 Q[NB][8];
  f32x4  S[NB][4];
  u32x4  P[NB][2];
  float  nm[NB], dl[NB], mx[NB];
  bf16x8 ones;
  unsigned kbase, vbase;
  unsigned vK0, vK1, vV, vWo;
};

template <int I, bool NT> DI void dma_piece(Desc& d) { d.so = dma<4096 * I, NT>(d.mb, d.so, d.step, (I & 1) ? d.v1 : d.v0, d.srd); }

template <int NB, int KB0, int K> DI void sm_op(Ctx<NB>& c) {
  constexpr int NU = NB * 4;
  constexpr int U = K / 5, pos = K % 5;
  if constexpr (pos < 4) {
    if constexpr (U < NU) {
      constexpr int blk = U % NB, q = U / NB, kb = KB0 + (q >> 1), r = 2 * (q & 1) + (pos & 1);
      float x = c.S[blk][kb][r];
      if constexpr (pos < 2) fma_ip(x, SCL, c.nm[blk]); else exp_ip(x);
      c.S[blk][kb][r] = x;
    }
  }
}
template <int NB, int KB0, int U> DI void cv_op(Ctx<NB>& c) {
  constexpr int blk = U % NB, q = U / NB, kb = KB0 + (q >> 1), r = 2 * (q & 1);
  c.P[blk][KB0 >> 1][q] = cvt_v(c.S[blk][kb][r], c.S[blk][kb][r + 1]);
}

template <int NB, bool NT> DI void phaseA(Ctx<NB>& c, unsigned slot, Desc& d, Prof& pf, int si) {
  PRE(si); wait_bar<24>(); POST(si); RT(si);
  const unsigned b0 = c.kbase + slot;
  unsigned kb4[4] = {b0, b0 ^ 64u, b0 ^ 128u, b0 ^ 192u};
  bf16x8 fr[4];
  auto rd = [&](auto F) AI { constexpr int f = F, kb = f >> 3, s = f & 7; fr[f & 3] = lds_b128(kb4[s & 3] + 256 * (s >> 2) + 8192 * kb); };
  rd(IC<0>{}); rd(IC<1>{}); rd(IC<2>{});
  constexpr int NU = NB * 4, NOPS = NU * 5 + 5;
  constexpr int GF = 22 * NB;
  FENCE();
  sfor<32>([&](auto F) AI {
    constexpr int f = F, kb = f >> 3, s = f & 7;
    sfor<NB>([&](auto Bk) AI {
      constexpr int blk = Bk, g = f * NB + blk;
      if constexpr (blk == 0) { if constexpr (s == 0) mf_va0(c.S[0][kb], fr[f & 3], c.Q[0][s]); else mf_va(c.S[0][kb], fr[f & 3], c.Q[0][s]); }
      else                    { if constexpr (s == 0) mf_vv0(c.S[blk][kb], fr[f & 3], c.Q[blk][s]); else mf_vv(c.S[blk][kb], fr[f & 3], c.Q[blk][s]); }
      if constexpr (blk == 0 && f + 3 < 32) rd(IC<f + 3>{});
      if constexpr (blk == 1 && (f & 3) == 1) dma_piece<(f >> 2), NT>(d);
      constexpr int lo = (g < GF) ? (g * NOPS) / GF : NOPS, hi = (g + 1 < GF) ? ((g + 1) * NOPS) / GF : NOPS;
      sfor<hi - lo>([&](auto J) AI {
        constexpr int k = lo + J;
        if constexpr (k % 5 < 4) sm_op<NB, 2, k>(c);
        else if constexpr (k / 5 >= 1 && k / 5 - 1 < NU) cv_op<NB, 2, k / 5 - 1>(c);
      });
      FENCE();
    });
  });
}

template <int NB> DI float rowmax16(const f32x4* s) {
  float m = max3f(s[0][0], s[0][1], s[0][2]);
  m = max3f(m, s[0][3], s[1][0]); m = max3f(m, s[1][1], s[1][2]); m = max3f(m, s[1][3], s[2][0]);
  m = max3f(m, s[2][1], s[2][2]); m = max3f(m, s[2][3], s[3][0]); m = max3f(m, s[3][1], s[3][2]);
  return max2f(m, s[3][3]);
}
DI float xl32(float v) { auto p = __builtin_amdgcn_permlane32_swap(__float_as_uint(v), __float_as_uint(v), false, false); return max2f(__uint_as_float(p[0]), __uint_as_float(p[1])); }
DI float xl16(float v) { auto p = __builtin_amdgcn_permlane16_swap(__float_as_uint(v), __float_as_uint(v), false, false); return max2f(__uint_as_float(p[0]), __uint_as_float(p[1])); }

template <int NB> DI void phaseB0(Ctx<NB>& c) {
  asm volatile("s_nop 7" ::: "memory");
  FENCE();
  sfor<NB>([&](auto Bk) AI { constexpr int blk = Bk; c.mx[blk] = rowmax16<NB>(c.S[blk]); });
  sfor<NB>([&](auto Bk) AI { constexpr int blk = Bk; c.nm[blk] = -SCL * xl16(xl32(c.mx[blk])); });
  sfor<NB * 20 + 5>([&](auto J) AI { constexpr int k = J;
    if constexpr (k % 5 < 4) sm_op<NB, 0, k>(c);
    else if constexpr (k / 5 >= 1 && k / 5 - 1 < NB * 4) cv_op<NB, 0, k / 5 - 1>(c); });
  sfor<NB>([&](auto Bk) AI { constexpr int blk = Bk;
    c.L[blk] = f32x4{0.f, 0.f, 0.f, 0.f};
    sfor<16>([&](auto Db) AI { c.O[blk][Db] = f32x4{0.f, 0.f, 0.f, 0.f}; tie_a4(c.O[blk][Db]); }); });
}

template <int NB, bool FINAL, bool NT> DI void phaseB(Ctx<NB>& c, unsigned slot, Desc& d, Prof& pf, int si) {
  PRE(si); wait_bar<24>(); POST(si); RT(si);
  const unsigned b0 = c.vbase + slot;
  bf16x8 fr[4];
  unsigned vbj[2];
  auto rd = [&](auto F) AI {
    constexpr int f = F, u = f >> 4, j = (f >> 1) & 7, hi = f & 1;
    if constexpr (hi == 0) vbj[j & 1] = b0 ^ (32u * j);
    fr[f & 3] = lds_tr2(vbj[j & 1] + 256 * hi + 16384 * u, vbj[j & 1] + 256 * hi + 16384 * u + 8192);
  };
  rd(IC<0>{}); rd(IC<1>{}); rd(IC<2>{});
  unsigned long long chg = 0;
  if constexpr (!FINAL) {
    asm volatile("s_nop 1" ::: "memory");
    sfor<NB>([&](auto Bk) AI { constexpr int blk = Bk; c.mx[blk] = rowmax16<NB>(c.S[blk]); });
  }
  FENCE();
  constexpr int NU = NB * 4;
  constexpr int GH = 17 * NB;
  sfor<2>([&](auto U_) AI {
    constexpr int u = U_;
    sfor<17>([&](auto FF) AI {
      constexpr int ff = FF;
      constexpr int f = 16 * u + ff - 1;
      sfor<NB>([&](auto Bk) AI {
        constexpr int blk = Bk, g = u * GH + ff * NB + blk;
        if constexpr (ff == 0) mf_vv(c.L[blk], c.ones, __builtin_bit_cast(bf16x8, c.P[blk][u]));
        else {
          constexpr int db = ((f >> 1) & 7) + 8 * (f & 1);
          mf_aa(c.O[blk][db], fr[f & 3], __builtin_bit_cast(bf16x8, c.P[blk][u]));
          if constexpr (blk == 0 && f + 3 < 32) rd(IC<f + 3>{});
          if constexpr (blk == 1 && (f & 3) == 1) dma_piece<(f >> 2), NT>(d);
        }
        if constexpr (FINAL) {
          constexpr int NOPS = NU * 5 + 5, GF = GH - 2;
          constexpr int lo = (g < GF) ? (g * NOPS) / GF : NOPS, hi = (g + 1 < GF) ? ((g + 1) * NOPS) / GF : NOPS;
          sfor<hi - lo>([&](auto J) AI { constexpr int k = lo + J;
            if constexpr (k % 5 < 4) sm_op<NB, 2, k>(c);
            else if constexpr (k / 5 >= 1 && k / 5 - 1 < NU) cv_op<NB, 2, k / 5 - 1>(c); });
        } else {
          if constexpr (g < NB) c.mx[g] = xl32(c.mx[g]);
          else if constexpr (g < 2 * NB) c.mx[g - NB] = xl16(c.mx[g - NB]);
          else if constexpr (g < 3 * NB) {
            constexpr int b = g - 2 * NB;
            const float t = fma_s(c.mx[b], SCL, c.nm[b]);
            const bool cond = t > THR;
            c.dl[b] = cond ? t : 0.f;
            c.nm[b] -= c.dl[b];
            chg |= __ballot(cond);
          } else {
            constexpr int G0 = 3 * NB + 1, G1 = 2 * GH - NB - 1, NOPS = NU * 4;
            constexpr int lo = (g < G0) ? 0 : ((g - G0 < G1 - G0) ? ((g - G0) * NOPS) / (G1 - G0) : NOPS);
            constexpr int hi = (g + 1 < G0) ? 0 : ((g + 1 - G0 < G1 - G0) ? ((g + 1 - G0) * NOPS) / (G1 - G0) : NOPS);
            sfor<hi - lo>([&](auto J) AI { constexpr int k = lo + J; sm_op<NB, 0, (k / 4) * 5 + (k % 4)>(c); });
            sfor<NU>([&](auto UU) AI {
              constexpr int un = UU;
              constexpr int gdone = G0 + ((4 * un + 4) * (G1 - G0) + NOPS - 1) / NOPS + 1;
              constexpr int gmin = GH + 1 + un;
              constexpr int gat = gdone > gmin ? gdone : gmin;
              if constexpr (g == (gat < 2 * GH - 1 ? gat : 2 * GH - 1)) cv_op<NB, 0, un>(c);
            });
          }
        }
        FENCE();
      });
    });
  });
  if constexpr (!FINAL) {
    if (__builtin_expect(chg != 0ull, 0)) {
      asm volatile("s_nop 7" ::: "memory");
      FENCE();
      sfor<NB>([&](auto Bk) AI { constexpr int blk = Bk;
        const float f = __builtin_amdgcn_exp2f(-c.dl[blk]);
        c.L[blk] *= f;
        sfor<16>([&](auto Db) AI { f32x4 o = c.O[blk][Db];
          o[0] = acc_mul(o[0], f); o[1] = acc_mul(o[1], f); o[2] = acc_mul(o[2], f); o[3] = acc_mul(o[3], f);
          c.O[blk][Db] = o; }); });
      FENCE();
      asm volatile("s_nop 3" ::: "memory");
    }
  }
}

template <int NB> struct Epi {
  bf16x8 of[NB][8];
  f32x4  y[2][NB][2];
  float  rl[NB];
  unsigned vP[NB];
  u32x4  srdP;
};
template <int NB, int BUF, int K> DI void drain_op(Epi<NB>& e, u32x4 (&t)[2], unsigned so) {
  constexpr int blk = K / 13, pos = K % 13;
  if constexpr (pos < 8) { float v = e.y[BUF][blk][pos >> 2][pos & 3]; e.y[BUF][blk][pos >> 2][pos & 3] = mul_v(v, e.rl[blk]); }
  else if constexpr (pos < 12) { constexpr int q = pos - 8; t[blk & 1][q] = cvt_v(e.y[BUF][blk][q >> 1][2 * (q & 1)], e.y[BUF][blk][q >> 1][2 * (q & 1) + 1]); }
  else st16_sc1(t[blk & 1], e.vP[blk], so, e.srdP);
}
template <int NB, int F> DI void yrd(bf16x8 (&fr)[4], unsigned b0) {
  constexpr int nb = F >> 3, s = F & 7; const unsigned a = (b0 ^ (32u * nb)) + 4096 * s; fr[F & 3] = lds_tr2(a, a + 2048);
}
template <int NB, int HF> DI void halfY(Epi<NB>& e, bf16x8 (&fr)[4], unsigned b0, unsigned so) {
  u32x4 t[2];
  constexpr int NOPS = NB * 13, G0 = 2, G1 = 16 * NB - 1;
  FENCE();
  sfor<16>([&](auto F) AI {
    constexpr int fl = F, f = 16 * HF + fl, nb = fl >> 3, s = f & 7;
    sfor<NB>([&](auto Bk) AI {
      constexpr int blk = Bk, g = fl * NB + blk;
      if constexpr (s == 0) mf_vv0(e.y[HF][blk][nb], fr[f & 3], e.of[blk][s]); else mf_vv(e.y[HF][blk][nb], fr[f & 3], e.of[blk][s]);
      if constexpr (blk == 0 && f + 3 < 32) yrd<NB, f + 3>(fr, b0);
      constexpr int lo = g < G0 ? 0 : (g >= G1 ? NOPS : ((g - G0) * NOPS) / (G1 - G0)), hi = g + 1 < G0 ? 0 : (g + 1 >= G1 ? NOPS : ((g + 1 - G0) * NOPS) / (G1 - G0));
      sfor<hi - lo>([&](auto J) AI { drain_op<NB, HF ^ 1, lo + J>(e, t, so); });
      FENCE();
    });
  });
}

struct Red {
  u32x4 srdF, srdR, srdO;
  unsigned tlo, thi;
  unsigned vF;
  unsigned vR, vO;
  unsigned lane;
};
DI void flag_set(const Red& r, unsigned line) {
  u32x2 tg = {r.tlo, r.thi};
  st8_sc1(tg, r.vF, line * 128u, r.srdF);
}
DI void poll_issue(const Red& r, unsigned line) {
  const unsigned vo = r.lane < 16u ? line * 128u + r.lane * 8u : OOB;
  asm volatile("buffer_load_dwordx2 a[126:127], %0, %1, 0 offen sc1" :: "v"(vo), "s"(r.srdF) : "memory", XCLOB);
}
DI bool poll_ok(const Red& r) {
  unsigned lo, hi;
  asm volatile("v_accvgpr_read_b32 %0, a126\n\tv_accvgpr_read_b32 %1, a127" : "=v"(lo), "=v"(hi) :: XCLOB);
  return __ballot(r.lane >= 16u || (lo == r.tlo && hi == r.thi)) == ~0ull;
}
DI void flag_wait(const Red& r, unsigned line) {
#pragma unroll 1
  for (int it = 0; it < (1 << 22); ++it) {
    poll_issue(r, line);
    wait_vm<0>();
    if (poll_ok(r)) break;
    __builtin_amdgcn_s_sleep(1);
  }
}
template <int R, int OFF> DI void pa_load(unsigned vo, unsigned so, const u32x4& srd) {
  asm volatile("buffer_load_dwordx4 a[%c0:%c1], %2, %3, %4 offen offset:%5 sc1" :: "i"(R), "i"(R + 3), "v"(vo), "s"(srd), "s"(so), "i"(OFF) : XCLOB); }
template <int R> DI void red_issue(const Red& r, unsigned c, unsigned idx) {
  const unsigned fr = (idx >> 1) * 14u + 2u * c + (idx & 1u);
  const unsigned so = rfl(fr * 1024u);
  sfor<8>([&](auto J) AI {
    const unsigned s2 = so + 2u * J * PARTB;
    pa_load<R + 8 * J, 0>(r.vR, s2, r.srdR);
    pa_load<R + 8 * J + 4, 256>(r.vR, s2, r.srdR);
  });
}
template <int R> DI void red_finish(const Red& r, const bf16x8& sel, unsigned c, unsigned idx) {
  f32x4 a0, a1;
  asm volatile("" ::: XCLOB);
  sfor<8>([&](auto J) AI {
    if constexpr (J == 0) { mf_vx0<R>(a0, sel); mf_vx0<R + 4>(a1, sel); }
    else { mf_vx<R + 8 * J>(a0, sel); mf_vx<R + 8 * J + 4>(a1, sel); }
  });
  asm volatile("s_nop 7\n\ts_nop 1" ::: "memory", XCLOB);
  FENCE();
  u32x4 t = {cvt_v(a0[0], a0[1]), cvt_v(a0[2], a0[3]), cvt_v(a1[0], a1[1]), cvt_v(a1[2], a1[3])};
  const unsigned so = rfl((idx >> 1) * (16u * 2u * DM * 2u) + (2u * c + (idx & 1u)) * 64u);
  st16(t, r.vO, so, r.srdO);
}

template <int NB>
DI void body(const __hip_bfloat16* __restrict__ x, const __hip_bfloat16* __restrict__ kc, const __hip_bfloat16* __restrict__ vc,
             const __hip_bfloat16* __restrict__ wq, const __hip_bfloat16* __restrict__ wo, char* __restrict__ ws, __hip_bfloat16* __restrict__ out,
             int br, int b, int h, unsigned jx, unsigned w, unsigned lane, unsigned lds0, uint64_t pc0) {
  const int grp = br * 8 + b;
  const unsigned g = lane >> 4, cc = lane & 15u, q4 = cc >> 2, p4 = lane & 3u, hi5 = lane >> 5, ch = lane & 31u;
  Ctx<NB> c;
  const u32x4 srdK = mk_srd((const char*)kc + ((size_t)(b * NH + h) * SK) * (DH * 2), SK * DH * 2);
  const u32x4 srdV = mk_srd((const char*)vc + ((size_t)(b * NH + h) * SK) * (DH * 2), SK * DH * 2);
  const u32x4 srdWq = mk_srd((const char*)wq + h * (DH * 2), DM * NH * DH * 2 - h * (DH * 2));
  const u32x4 srdWo = mk_srd((const char*)wo + (size_t)h * DH * DM * 2, DH * DM * 2);
  const u32x4 srdX = mk_srd((const char*)x + (size_t)b * SQ * DM * 2, SQ * DM * 2);
  const u32x4 srdF = mk_srd(ws + FLAG_OFF + (size_t)grp * 4096, 4096);
  const unsigned rb[3] = {2u * w, 2u * w + 1u, 8u + w};
  Prof pf;
  POST(0); RT(0);

  unsigned dummy = 0;
  u32x2 ep;
  ld8_sc1(ep, 28u * 128u, srdF);
  const unsigned vWq0 = 8192u * hi5 + 16u * (ch ^ (2u * (2u * (w & 1u) + hi5))), vWq1 = vWq0 ^ (16u * 8u);
  const unsigned mbw = rfl(lds0 + 1024u * (w & 1u));
  unsigned vx[NB];
  sfor<NB>([&](auto Bk) AI { vx[Bk] = (16u * rb[Bk] + (lane >> 2)) * (DM * 2) + 16u * (p4 ^ (2u * hi5)); });
  auto xld = [&](auto K_) AI { constexpr int k = K_, ks = k / NB, bk = k % NB; xa_load<88 + 4 * k, 64 * ks>(vx[bk], srdX); };
  if constexpr (NB == 2) {
    unsigned so = rfl(2u * (w & 1u) * 8192u);
    sfor<16>([&](auto I) AI { constexpr int i = I; so = dma<SLOT * 1 + 2048 * i, false>(mbw, so, 4u * 8192u, ((i >> 1) & 1) ? vWq1 : vWq0, srdWq); });
  }
  POST(53);
  sfor<6 * NB>(xld);
  POST(54);
  if constexpr (NB == 2) {
    unsigned so = rfl((64u + 2u * (w & 1u)) * 8192u);
    sfor<16>([&](auto I) AI { constexpr int i = I; so = dma<SLOT * 0 + 2048 * i, false>(mbw, so, 4u * 8192u, ((i >> 1) & 1) ? vWq1 : vWq0, srdWq); });
  } else {
    if (w == 0) {
      sfor<3>([&](auto I) AI {
        const unsigned idx = 64u * I + lane;
        const unsigned vo = (64u * (2u + (idx >> 5)) + 8u * b + ((idx >> 2) & 7u)) * 8192u + (idx & 3u) * 128u;
        pf_load(dummy, idx < 160u ? vo : OOB, srdWq);
      });
    } else {
      const u32x4 srdC = mk_srd((const void*)(pc0 & ~127ull), CODE_PF);
      pf_load(dummy, lane < CODE_PF / 4096u ? (jx * (CODE_PF / 4096u) + lane) * 128u : OOB, srdC);
    }
  }

  PRE(1);
  c.kbase = lds0 + 512u * cc + 16u * (g ^ (cc & 3u)) + 64u * (cc >> 2);
  c.vbase = lds0 + 512u * (4u * g + q4) + 32u * (4u * (g & 1u) + q4) + 8u * p4;
  c.vK0 = 512u * hi5 + 16u * (ch ^ hi5 ^ (2u * w));
  c.vK1 = 512u * hi5 + 16u * (ch ^ hi5 ^ (2u * w + 8u));
  c.vV  = 512u * hi5 + 16u * (ch ^ (2u * (2u * w + hi5)));
  c.vWo = 896u * (lane >> 3) + 16u * ((lane & 7u) ^ (2u * ((lane >> 4) & 3u)));
  { u32x4 o = {0x3F803F80u, 0x3F803F80u, 0x3F803F80u, 0x3F803F80u}; c.ones = __builtin_bit_cast(bf16x8, o); tie_v8(c.ones); }
  const unsigned mb4 = rfl(lds0 + 1024u * w);

  auto desc = [&](int n) AI {
    Desc d; d.mb = mb4 + (unsigned)((n + 2) % 5) * SLOT;
    if (n >= 32)      { d.srd = srdWo; d.so = 7168u * w + 128u * (unsigned)(n - 32); d.step = 4u * 7168u; d.v0 = d.v1 = c.vWo; }
    else if ((n & 1) && n != 31) { d.srd = srdK; d.so = (unsigned)((n + 1) >> 1) * SLOT + 1024u * w; d.step = 4096u; d.v0 = c.vK0; d.v1 = c.vK1; }
    else              { d.srd = srdV; d.so = (unsigned)(n == 31 ? 15 : ((n - 2) >> 1)) * SLOT + 1024u * w; d.step = 4096u; d.v0 = d.v1 = c.vV; }
    return d;
  };

  unsigned tlo, thi;
  {
    f32x4 qa[NB][16];
    u32x4 qt[NB][8];
    const unsigned wbase = lds0 + 512u * (8u * g + q4) + 32u * (q4 + 4u * (g & 1u)) + 8u * p4;
    const unsigned pa = 4u * (4u * cc + (g ^ (2u * (cc >> 3))));
    unsigned vkv[4];
    auto qcv = [&](auto BB, auto SP, auto K_) AI {
      constexpr int bb = BB, sp = SP, k = K_;
      if constexpr (k < 4) qt[bb][sp][k] = cvt_v(qa[bb][2 * sp + (k >> 1)][2 * (k & 1)], qa[bb][2 * sp + (k >> 1)][2 * (k & 1) + 1]);
      else { unsigned x0 = qt[bb][sp][k & 1], x1 = qt[bb][sp][2 + (k & 1)];
        if constexpr (k < 6) swap32(x0, x1); else swap16(x0, x1);
        qt[bb][sp][k & 1] = x0; qt[bb][sp][2 + (k & 1)] = x1; }
    };
    sfor<7>([&](auto C_) AI {
      constexpr int C = C_;
      PRE(2 + C);
      if constexpr (NB == 3) { constexpr int n[7] = {6, 12, 18, 28, 38, 48, 63}; wait_vm<n[C]>(); }
      else { constexpr int n[7] = {20, 8, 8, 0, 0, 0, 0}; wait_vm<n[C]>(); }
      if constexpr (C == 0) {
        tie_v2(ep);
        tlo = rfl(ep[0]) + 1u; thi = rfl(ep[1]) + (tlo == 0u ? 1u : 0u);
        sfor<8 * NB>([&](auto K) AI { xa_perm<88 + (int)K>(pa); });
      }
      if constexpr (C == 2) tie_v(dummy);
      asm volatile("s_waitcnt lgkmcnt(0)\n\ts_barrier" ::: "memory", XCLOB);
      POST(2 + C); RT(2 + C);
      const unsigned b0 = wbase + SLOT * ((C + 1) & 1);
      constexpr int PD = 5;
      bf16x8 fr[PD + 1];
      auto rd = [&](auto F) AI { constexpr int f = F, be = f >> 1, u = f & 1;
        const unsigned a = (b0 ^ (32u * (be & 7))) + 256 * (be >> 3) + 16384 * u; fr[f % (PD + 1)] = lds_tr2(a, a + 2048); };
      sfor<PD>(rd);
      unsigned so = 0; Desc d3;
      if constexpr (NB == 2 && C >= 1 && C <= 5) so = rfl((64u * (C + 1) + 2u * (w & 1u)) * 8192u);
      if constexpr (NB == 3 && C >= 2 && C <= 4) {
        so = rfl((C == 3 ? SLOT : 0u) + 1024u * w);
        if constexpr (C == 2) sfor<4>([&](auto I) AI { vkv[I] = 512u * hi5 + 16u * (ch ^ hi5 ^ ((4u * I + 2u * w) & 15u)); });
        if constexpr (C == 4) sfor<2>([&](auto I) AI { vkv[I] = 512u * hi5 + 16u * (ch ^ (2u * ((2u * (2u * I + w) + hi5) & 7u))); });
      }
      if constexpr (C == 6) d3 = desc(3);
      FENCE();
      sfor<32>([&](auto F) AI {
        constexpr int f = F, be = f >> 1, u = f & 1;
        sfor<NB>([&](auto Bk) AI {
          constexpr int blk = Bk, gp = f * NB + blk;
          constexpr int XR = 88 + 4 * ((2 * C + u) * NB + blk);
          if constexpr (C == 0 && u == 0) mf_vx0<XR>(qa[blk][be], fr[f % (PD + 1)]); else mf_vx<XR>(qa[blk][be], fr[f % (PD + 1)]);
          if constexpr (blk == 0 && f + PD < 32) rd(IC<f + PD>{});
          if constexpr (NB == 2 && C >= 1 && C <= 5 && gp < 16) so = dma<SLOT * (C & 1) + 2048 * gp, false>(mbw, so, 4u * 8192u, ((gp >> 1) & 1) ? vWq1 : vWq0, srdWq);
          if constexpr (C <= 1 && NB == 3 && (gp & 7) == 1 && (gp >> 3) < 4 * NB) xld(IC<(6 + 4 * C) * NB + (gp >> 3)>{});
          if constexpr (C <= 1 && NB == 2 && gp >= 17 && ((gp - 17) & 3) == 0 && ((gp - 17) >> 2) < 4 * NB) xld(IC<(6 + 4 * C) * NB + ((gp - 17) >> 2)>{});
          if constexpr (NB == 3 && C >= 2 && C <= 4 && blk == 1 && (f & 1) == 0) {
            constexpr int i = f >> 1;
            if constexpr (C < 4) so = dma<SLOT * C + 2048 * i, true>(mbw, so, 2048u, vkv[i & 3], srdK);
            else                 so = dma<SLOT * 4 + 2048 * i, true>(mbw, so, 2048u, vkv[i & 1], srdV);
          }
          if constexpr (C == 6 && blk == 1 && (f & 3) == 1) dma_piece<(f >> 2), true>(d3);
          if constexpr (C <= 5 && (gp & 3) == 2 && (gp >> 2) < 8 * NB) xa_perm<88 + 4 * (2 * C + 2) * NB + (gp >> 2)>(pa);
          if constexpr (C == 6 && gp >= 8 * NB) {
            constexpr int k = gp - 8 * NB, un = k >> 2, q = k & 3, sp2 = un / NB, bb = un % NB;
            qcv(IC<bb>{}, IC<sp2>{}, IC<2 * q>{}); qcv(IC<bb>{}, IC<sp2>{}, IC<2 * q + 1>{});
          }
          FENCE();
        });
      });
    });
    PRE(9);
    asm volatile("s_nop 7" ::: "memory", XCLOB);
    FENCE();
    sfor<4>([&](auto Q_) AI { sfor<2 * NB>([&](auto U_) AI { constexpr int q = Q_, un = 6 * NB + U_, sp2 = un / NB, bb = un % NB;
        qcv(IC<bb>{}, IC<sp2>{}, IC<2 * q>{}); qcv(IC<bb>{}, IC<sp2>{}, IC<2 * q + 1>{}); }); });
    sfor<NB>([&](auto Bk) AI { sfor<8>([&](auto Sp) AI { c.Q[Bk][Sp] = __builtin_bit_cast(bf16x8, qt[Bk][Sp]); }); });
    sfor<8>([&](auto Sp) AI { tie_a(c.Q[0][Sp]); });
    sfor<NB>([&](auto Bk) AI { c.nm[Bk] = 0.f; sfor<4>([&](auto Kb) AI { c.S[Bk][Kb] = f32x4{0.f, 0.f, 0.f, 0.f}; }); });
    FENCE();
    asm volatile("s_nop 1" ::: "memory");
  }

  {
    Desc d = desc(4);
    phaseA<NB, true>(c, 2u * SLOT, d, pf, 10);
    PRE(11);
    phaseB0<NB>(c);
  }
  unsigned sl = 3;
  for (int t = 1; t < 16; ++t) {
    { Desc d = desc(2 * t + 3); phaseA<NB, true>(c, sl * SLOT, d, pf, 10 + 2 * t); }
    sl = sl == 4 ? 0 : sl + 1;
    { Desc d = desc(2 * t + 4); phaseB<NB, false, true>(c, sl * SLOT, d, pf, 11 + 2 * t); }
    sl = sl == 4 ? 0 : sl + 1;
  }
  { Desc d = desc(35); phaseB<NB, true, true>(c, sl * SLOT, d, pf, 42); }
  PRE(43);
  sl = sl == 4 ? 0 : sl + 1;

  asm volatile("s_nop 7" ::: "memory");
  FENCE();
  Epi<NB> e;
  sfor<NB>([&](auto Bk) AI { constexpr int blk = Bk;
    tie_v4(c.L[blk]);
    e.rl[blk] = __builtin_amdgcn_rcpf(c.L[blk][0]);
    sfor<8>([&](auto Sp) AI { constexpr int sp = Sp;
      const f32x4 o0 = c.O[blk][2 * sp], o1 = c.O[blk][2 * sp + 1];
      const float a0 = acc_rd(o0[0]), a1 = acc_rd(o0[1]), a2 = acc_rd(o0[2]), a3 = acc_rd(o0[3]);
      const float a4 = acc_rd(o1[0]), a5 = acc_rd(o1[1]), a6 = acc_rd(o1[2]), a7 = acc_rd(o1[3]);
      u32x4 t = {cvt_v(a0, a1), cvt_v(a2, a3), cvt_v(a4, a5), cvt_v(a6, a7)};
      e.of[blk][sp] = __builtin_bit_cast(bf16x8, t); });
    sfor<2>([&](auto Nb) AI { e.y[1][blk][Nb] = f32x4{0.f, 0.f, 0.f, 0.f}; tie_v4(e.y[1][blk][Nb]); }); });
  FENCE();
  asm volatile("s_nop 1" ::: "memory");
  POST(43);
  const unsigned laneE = lane_id(), gE = laneE >> 4, ccE = laneE & 15u, q4E = ccE >> 2;
  const unsigned obase = lds0 + 128u * (4u * gE + q4E) + 32u * ((2u * gE + (q4E >> 1)) & 3u) + 8u * (laneE & 3u);
  e.srdP = mk_srd(ws + (size_t)(grp * NH + h) * PARTB, PARTB);
  sfor<NB>([&](auto Bk) AI { e.vP[Bk] = rb[Bk] * (14u * 1024u) + laneE * 16u; });
  Red r;
  r.srdF = srdF; r.tlo = tlo; r.thi = thi; r.lane = laneE;
  r.vF = laneE == 0u ? (unsigned)h * 8u : OOB;
  r.srdR = mk_srd(ws + (size_t)grp * NH * PARTB, NH * PARTB);
  r.srdO = mk_srd((char*)out + ((size_t)b * SQ * 2 * DM + br * DM) * 2, (SQ * 2 * DM - br * DM) * 2);
  r.vR = (gE & 1u) * PARTB + (32u * (gE >> 1) + ccE) * 16u;
  r.vO = ccE * (2u * DM * 2u) + 32u * (gE & 1u) + 16u * (gE >> 1);

  asm volatile("; EPI_BEGIN" ::: XCLOB);
  u32x4 sv; sfor<4>([&](auto I) AI { constexpr int i = I; const unsigned m7 = ccE & 7u; const bool on = (gE >> 1) == (ccE >> 3);
    sv[i] = (on && m7 == 2u * i) ? 0x00003F80u : ((on && m7 == 2u * i + 1u) ? 0x3F800000u : 0u); });
  bf16x8 sel = __builtin_bit_cast(bf16x8, sv); tie_v8(sel);
  const unsigned c0 = (unsigned)h >> 2, ib = 16u + ((unsigned)h & 3u);
  const unsigned iw = w == 2u ? (unsigned)h : ib;
  const unsigned lw = w == 2u ? c0 : (ib >> 1) - 8u;
  PRE(44); wait_bar<24>(); POST(44); RT(44);
#pragma unroll 1
  for (int p = 0; p < 7; ++p) {
    const unsigned b0 = obase + sl * SLOT;
    bf16x8 fr[4];
    yrd<NB, 0>(fr, b0); yrd<NB, 1>(fr, b0); yrd<NB, 2>(fr, b0);
    halfY<NB, 0>(e, fr, b0, p == 0 ? OOB : (unsigned)(2 * p - 1) * 1024u);
    wait_vm<2 * NB>();
    if (p >= 2) flag_set(r, (unsigned)(p - 2) * 4u + w);
    if (p <= 2) { Desc d = desc(36 + p); sfor<8>([&](auto I) AI { dma_piece<I, false>(d); }); }
    if constexpr (NB == 2) {
      if (p >= 3) {
        const unsigned cf = p - 5, cl = p - 4, cp = p - 3;
        if (p >= 5 && (w == 2u || c0 == cf)) red_finish<128>(r, sel, cf, iw);
        if (p >= 4 && (w == 2u || c0 == cl)) {
          if (!poll_ok(r)) flag_wait(r, cl * 4u + lw);
          red_issue<128>(r, cl, iw);
        }
        if (p <= 5 && (w == 2u || c0 == cp)) poll_issue(r, cp * 4u + lw);
      }
    }
    halfY<NB, 1>(e, fr, b0, (unsigned)(2 * p) * 1024u);
    sl = sl == 4 ? 0 : sl + 1;
    PRE(45 + p);
    if (p < 4) asm volatile("s_barrier" ::: "memory");
    POST(45 + p); RT(45 + p);
  }
  {
    asm volatile("s_nop 7" ::: "memory");
    FENCE();
    u32x4 t[2];
    sfor<NB * 13>([&](auto K) AI { drain_op<NB, 1, K>(e, t, 13u * 1024u); });
  }
  wait_vm<2 * NB>();
  flag_set(r, 5u * 4u + w);
  wait_vm<1>();
  flag_set(r, 6u * 4u + w);
  POST(52); RT(52);
  if constexpr (NB == 2) {
    const unsigned ca = w + 1u, cb = w + 3u;
    flag_wait(r, ca * 4u + c0); red_issue<192>(r, ca, h);
    if (w == 2u || c0 == 2u) red_finish<128>(r, sel, 2u, iw);
    POST(55); RT(55);
    flag_wait(r, cb * 4u + c0); red_issue<128>(r, cb, h);
    POST(56); RT(56);
    if (w == 2u && h == 0) { u32x2 tg = {tlo, thi}; st8_sc1(tg, laneE == 0u ? 0u : OOB, 28u * 128u, srdF); }
    red_finish<192>(r, sel, ca, h);
    wait_vm<1>();
    red_finish<128>(r, sel, cb, h);
  } else {
    if (w == 0u) {
      const unsigned cB = c0 == 3u ? 3u : c0 + 4u;
      flag_wait(r, cB * 4u + (ib >> 1) - 8u); red_issue<128>(r, cB, ib);
      wait_vm<0>();
      red_finish<128>(r, sel, cB, ib);
    }
  }
}

__global__ __launch_bounds__(256, 1)
void attn_kernel(const __hip_bfloat16* __restrict__ xA, const __hip_bfloat16* __restrict__ kA, const __hip_bfloat16* __restrict__ vA,
                 const __hip_bfloat16* __restrict__ wqA, const __hip_bfloat16* __restrict__ woA,
                 const __hip_bfloat16* __restrict__ xB, const __hip_bfloat16* __restrict__ kB, const __hip_bfloat16* __restrict__ vB,
                 const __hip_bfloat16* __restrict__ wqB, const __hip_bfloat16* __restrict__ woB, char* __restrict__ ws, __hip_bfloat16* __restrict__ out) {
  extern __shared__ __attribute__((aligned(16))) char lds[];
  uint64_t pc0; asm volatile("s_getpc_b64 %0" : "=s"(pc0));
  const unsigned blk = blockIdx.x, xcd = blk & 7u, j = blk >> 3;
  const int br = xcd & 1u, h = (xcd >> 1) * 4 + (j & 3u), b = j >> 2;
  const unsigned lane = threadIdx.x & 63u, w = rfl(threadIdx.x >> 6);
  const unsigned lds0 = (unsigned)(size_t)lds;
  const __hip_bfloat16* x  = br ? xB : xA;
  const __hip_bfloat16* kc = br ? kB : kA;
  const __hip_bfloat16* vc = br ? vB : vA;
  const __hip_bfloat16* wq = br ? wqB : wqA;
  const __hip_bfloat16* wo = br ? woB : woA;
  if (w < 2) body<3>(x, kc, vc, wq, wo, ws, out, br, b, h, j, w, lane, lds0, pc0);
  else       body<2>(x, kc, vc, wq, wo, ws, out, br, b, h, j, w, lane, lds0, pc0);
}

extern "C" void kernel_launch(void* const* d_in, const int* in_sizes, int n_in, void* d_out, int out_size, void* d_ws, size_t ws_size,
                              hipStream_t stream) {
  (void)in_sizes; (void)n_in; (void)out_size; (void)ws_size;
  typedef const __hip_bfloat16* P;
  (void)hipFuncSetAttribute((const void*)attn_kernel, hipFuncAttributeMaxDynamicSharedMemorySize, 163840);
  attn_kernel<<<256, 256, 163840, stream>>>((P)d_in[0], (P)d_in[1], (P)d_in[2], (P)d_in[3], (P)d_in[4],
                                            (P)d_in[5], (P)d_in[6], (P)d_in[7], (P)d_in[8], (P)d_in[9], (char*)d_ws, (__hip_bfloat16*)d_out);
}
```

```cpp
#include <hip/hip_runtime.h>
#include <hip/hip_bf16.h>
#include <utility>
#include <stdint.h>

typedef float    f32x4  __attribute__((ext_vector_type(4)));
typedef float    f32x2  __attribute__((ext_vector_type(2)));
typedef __bf16   bf16x8 __attribute__((ext_vector_type(8)));
typedef __bf16   bf16x4 __attribute__((ext_vector_type(4)));
typedef __bf16   bf16x2 __attribute__((ext_vector_type(2)));
typedef unsigned u32x4  __attribute__((ext_vector_type(4)));
typedef unsigned u32x2  __attribute__((ext_vector_type(2)));

#define DI __device__ __forceinline__
#define LDSP __attribute__((address_space(3)))
#define FENCE() __builtin_amdgcn_sched_barrier(0)
#define AI __attribute__((always_inline))

template <class F, int... Is> DI void sfor_(F&& f, std::integer_sequence<int, Is...>) { (f(std::integral_constant<int, Is>{}), ...); }
template <int N, class F> DI void sfor(F&& f) { sfor_(f, std::make_integer_sequence<int, N>{}); }
template <int V> using IC = std::integral_constant<int, V>;

constexpr int SQ = 160, DM = 448, NH = 16, SK = 1024, DH = 256;
constexpr unsigned SLOT = 32768u;
constexpr float SCL = 0.09016844005556021f;
constexpr float THR = 8.0f;
constexpr unsigned SRD3 = 0x00020000u;
constexpr unsigned PARTB = 140u * 1024u;
constexpr size_t FLAG_OFF = 40u << 20;
constexpr unsigned OOB = 0x40000000u;
constexpr unsigned CODE_PF = 64u * 1024u;

DI unsigned lane_id() { unsigned l; asm volatile("v_mbcnt_lo_u32_b32 %0, -1, 0\n\tv_mbcnt_hi_u32_b32 %0, -1, %0" : "=v"(l)); return l; }
DI unsigned rfl(unsigned x) { return __builtin_amdgcn_readfirstlane(x); }
DI u32x4 mk_srd(const void* p, unsigned bytes) {
  const uint64_t a = (uint64_t)p;
  u32x4 r; r[0] = rfl((unsigned)a); r[1] = rfl((unsigned)(a >> 32)); r[2] = bytes; r[3] = SRD3; return r;
}
DI unsigned cvtpk(float lo, float hi) { f32x2 v = {lo, hi}; bf16x2 b = __builtin_convertvector(v, bf16x2); return __builtin_bit_cast(unsigned, b); }
DI float max3f(float a, float b, float c) { float r; asm("v_max3_f32 %0, %1, %2, %3" : "=v"(r) : "v"(a), "v"(b), "v"(c)); return r; }
DI float max2f(float a, float b) { float r; asm("v_max_f32_e32 %0, %1, %2" : "=v"(r) : "v"(a), "v"(b)); return r; }
DI float fma_s(float a, float b, float c) { float r; asm("v_fma_f32 %0, %1, %2, %3" : "=v"(r) : "v"(a), "s"(b), "v"(c)); return r; }
DI void fma_ip(float& x, float s, float c) { asm volatile("v_fma_f32 %0, %0, %1, %2" : "+v"(x) : "s"(s), "v"(c)); }
DI void exp_ip(float& x) { asm volatile("v_exp_f32_e32 %0, %0" : "+v"(x)); }
DI unsigned cvt_v(float lo, float hi) { unsigned r; asm volatile("v_cvt_pk_bf16_f32 %0, %1, %2" : "=v"(r) : "v"(lo), "v"(hi)); return r; }
DI float mul_v(float a, float b) { float r; asm volatile("v_mul_f32_e32 %0, %1, %2" : "=v"(r) : "v"(a), "v"(b)); return r; }

DI void mf_vv(f32x4& d, const bf16x8& a, const bf16x8& b) { asm volatile("v_mfma_f32_16x16x32_bf16 %0, %1, %2, %0" : "+v"(d) : "v"(a), "v"(b)); }
DI void mf_vv0(f32x4& d, const bf16x8& a, const bf16x8& b) { asm volatile("v_mfma_f32_16x16x32_bf16 %0, %1, %2, 0" : "=&v"(d) : "v"(a), "v"(b)); }

template <int MO, bool NT> DI unsigned dma(unsigned mb, unsigned so, unsigned step, unsigned voff, const u32x4& srd) {
  unsigned nso;
  if constexpr (NT) asm volatile("s_add_u32 m0, %1, %2\n\ts_add_u32 %0, %3, %4\n\tbuffer_load_dwordx4 %5, %6, %3 offen nt lds" : "=&s"(nso) : "s"(mb), "i"(MO), "s"(so), "s"(step), "v"(voff), "s"(srd) : "scc");
  else              asm volatile("s_add_u32 m0, %1, %2\n\ts_add_u32 %0, %3, %4\n\tbuffer_load_dwordx4 %5, %6, %3 offen lds"    : "=&s"(nso) : "s"(mb), "i"(MO), "s"(so), "s"(step), "v"(voff), "s"(srd) : "scc");
  return nso;
}
DI bf16x8 lds_b128(unsigned addr) { return *(const LDSP bf16x8*)addr; }
DI bf16x8 lds_tr2(unsigned addr0, unsigned addr1) {
  bf16x4 lo = __builtin_amdgcn_ds_read_tr16_b64_v4bf16((LDSP bf16x4*)addr0);
  bf16x4 hi = __builtin_amdgcn_ds_read_tr16_b64_v4bf16((LDSP bf16x4*)addr1);
  return __builtin_shufflevector(lo, hi, 0, 1, 2, 3, 4, 5, 6, 7);
}
DI bf16x4 lds_tr(unsigned addr) { return __builtin_amdgcn_ds_read_tr16_b64_v4bf16((LDSP bf16x4*)addr); }
DI bf16x8 cat8(const bf16x4& lo, const bf16x4& hi) { return __builtin_shufflevector(lo, hi, 0, 1, 2, 3, 4, 5, 6, 7); }
DI void pf_load(unsigned& dummy, unsigned vo, const u32x4& srd) { asm volatile("buffer_load_dword %0, %1, %2, 0 offen" : "+v"(dummy) : "v"(vo), "s"(srd)); }
DI void ld8_sc1(u32x2& d, unsigned vo, const u32x4& srd) { asm volatile("buffer_load_dwordx2 %0, %1, %2, 0 offen sc1" : "=v"(d) : "v"(vo), "s"(srd)); }
DI void st8_sc1(const u32x2& d, unsigned vo, unsigned so, const u32x4& srd) { asm volatile("buffer_store_dwordx2 %0, %1, %2, %3 offen sc1" :: "v"(d), "v"(vo), "s"(srd), "s"(so) : "memory"); }
DI void st16_sc1(const u32x4& d, unsigned vo, unsigned so, const u32x4& srd) { asm volatile("buffer_store_dwordx4 %0, %1, %2, %3 offen sc1" :: "v"(d), "v"(vo), "s"(srd), "s"(so) : "memory"); }
DI void st16(const u32x4& d, unsigned vo, unsigned so, const u32x4& srd) { asm volatile("buffer_store_dwordx4 %0, %1, %2, %3 offen" :: "v"(d), "v"(vo), "s"(srd), "s"(so) : "memory"); }
#define XCLOB "a0","a1","a2","a3","a4","a5","a6","a7","a8","a9","a10","a11","a12","a13","a14","a15","a16","a17","a18","a19","a20","a21","a22","a23", \
  "a24","a25","a26","a27","a28","a29","a30","a31","a32","a33","a34","a35","a36","a37","a38","a39","a40","a41","a42","a43","a44","a45","a46","a47", \
  "a48","a49","a50","a51","a52","a53","a54","a55","a56","a57","a58","a59","a60","a61","a62","a63","a64","a65","a66","a67","a68","a69","a70","a71", \
  "a72","a73","a74","a75","a76","a77","a78","a79","a80","a81","a82","a83","a84","a85","a86","a87","a88","a89","a90","a91","a92","a93","a94","a95", \
  "a96","a97","a98","a99","a100","a101","a102","a103","a104","a105","a106","a107","a108","a109","a110","a111","a112","a113","a114","a115","a116","a117","a118","a119", \
  "a120","a121","a122","a123","a124","a125","a126","a127","a128","a129","a130","a131","a132","a133","a134","a135","a136","a137","a138","a139","a140","a141","a142","a143", \
  "a144","a145","a146","a147","a148","a149","a150","a151","a152","a153","a154","a155","a156","a157","a158","a159","a160","a161","a162","a163","a164","a165","a166","a167", \
  "a168","a169","a170","a171","a172","a173","a174","a175","a176","a177","a178","a179","a180","a181","a182","a183","a184","a185","a186","a187","a188","a189","a190","a191", \
  "a192","a193","a194","a195","a196","a197","a198","a199","a200","a201","a202","a203","a204","a205","a206","a207","a208","a209","a210","a211","a212","a213","a214","a215", \
  "a216","a217","a218","a219","a220","a221","a222","a223","a224","a225","a226","a227","a228","a229","a230","a231","a232","a233","a234","a235","a236","a237","a238","a239", \
  "a240","a241","a242","a243","a244","a245","a246","a247","a248","a249","a250","a251","a252","a253","a254","a255"
template <int R, int OFF> DI void xa_load(unsigned vo, const u32x4& srd) {
  asm volatile("buffer_load_dwordx4 a[%c0:%c1], %2, %3, 0 offen offset:%4" :: "i"(R), "i"(R + 3), "v"(vo), "s"(srd), "i"(OFF) : XCLOB); }
template <int R> DI void xa_perm(unsigned pa) { asm volatile("ds_bpermute_b32 a%c0, %1, a%c0" :: "i"(R), "v"(pa) : XCLOB); }
template <int R> DI void mf_vx(f32x4& d, const bf16x8& a) { asm volatile("v_mfma_f32_16x16x32_bf16 %0, %1, a[%c2:%c3], %0" : "+v"(d) : "v"(a), "i"(R), "i"(R + 3)); }
template <int R> DI void mf_vx0(f32x4& d, const bf16x8& a) { asm volatile("v_mfma_f32_16x16x32_bf16 %0, %1, a[%c2:%c3], 0" : "=&v"(d) : "v"(a), "i"(R), "i"(R + 3)); }
template <int NB> constexpr int XREG(int ks, int blk) { return NB == 3 ? (ks < 10 ? 12 * ks : (ks < 12 ? 192 + 12 * (ks - 10) : 224 + 12 * (ks - 12))) + 4 * blk : 8 * ks + 4 * blk; }
constexpr int OREG(int blk, int db) { return 64 * blk + 4 * db; }
template <int NB> constexpr int QREG(int sp) { return (NB == 3 ? 192 : 128) + 4 * sp; }
template <int R> DI void mf_o(const bf16x8& a, const bf16x8& b) { asm volatile("v_mfma_f32_16x16x32_bf16 a[%c2:%c3], %0, %1, a[%c2:%c3]" :: "v"(a), "v"(b), "i"(R), "i"(R + 3)); }
template <int R> DI void acc_zero() { asm volatile("v_accvgpr_write_b32 a%c0, 0" :: "i"(R)); }
template <int R> DI void acc_wr(unsigned v) { asm volatile("v_accvgpr_write_b32 a%c1, %0" :: "v"(v), "i"(R)); }
template <int R> DI float acc_rd() { float r; asm volatile("v_accvgpr_read_b32 %0, a%c1" : "=v"(r) : "i"(R)); return r; }
template <int R> DI void acc_mul(float f) { float t; asm volatile("v_accvgpr_read_b32 %0, a%c2\n\tv_mul_f32_e32 %0, %1, %0\n\tv_accvgpr_write_b32 a%c2, %0" : "=&v"(t) : "v"(f), "i"(R)); }
DI void pfa_load(unsigned vo, unsigned so, const u32x4& srd) { asm volatile("buffer_load_dword a255, %0, %1, %2 offen" :: "v"(vo), "s"(srd), "s"(so)); }
DI unsigned mov_v(unsigned a) { unsigned r; asm volatile("v_mov_b32 %0, %1" : "=v"(r) : "v"(a)); return r; }
DI void swap32(unsigned& a, unsigned& b) { asm volatile("v_permlane32_swap_b32 %0, %1" : "+v"(a), "+v"(b)); }
DI void swap16(unsigned& a, unsigned& b) { asm volatile("v_permlane16_swap_b32 %0, %1" : "+v"(a), "+v"(b)); }
DI void tie_v8(bf16x8& d) { asm volatile("" : "+v"(d)); }
DI void tie_v4(f32x4& d) { asm volatile("" : "+v"(d)); }
DI void tie_v2(u32x2& d) { asm volatile("" : "+v"(d)); }
DI void tie_v(unsigned& d) { asm volatile("" : "+v"(d)); }
template <int N> DI void wait_vm() { asm volatile("s_waitcnt vmcnt(%0)" :: "i"(N) : "memory"); }
template <int N> DI void wait_bar() { asm volatile("s_waitcnt vmcnt(%0) lgkmcnt(0)\n\ts_barrier" :: "i"(N) : "memory", XCLOB); }

#define RTL(i)
struct Prof {};
#define PRE(i)
#define POST(i)
#define RT(i)

struct Desc { u32x4 srd; unsigned so, inc, ginc, mb; unsigned v[4]; };

template <int NB> struct Ctx {
  f32x4  L[NB];
  bf16x8 Q[NB][8];
  f32x4  S[NB][4];
  u32x4  P[NB][2];
  float  nm[NB], thr[NB], dl[NB], mx[NB];
  bf16x8 ones;
  unsigned kbase, vbase;
};

template <int I> DI void dma_m0(const Desc& d) { asm volatile("s_add_u32 m0, %0, %1" :: "s"(d.mb), "i"(16384 * (I >> 2)) : "scc"); }
template <int I, bool NT> DI void dma_ld(const Desc& d) {
  if constexpr (NT) asm volatile("buffer_load_dwordx4 %0, %1, %2 offen offset:%3 nt lds" :: "v"(d.v[I & 3]), "s"(d.srd), "s"(d.so), "i"(1024 * (I & 3)));
  else              asm volatile("buffer_load_dwordx4 %0, %1, %2 offen offset:%3 lds"    :: "v"(d.v[I & 3]), "s"(d.srd), "s"(d.so), "i"(1024 * (I & 3)));
}
template <int I> DI void dma_adv(Desc& d) { if constexpr (I < 7) asm volatile("s_add_u32 %0, %0, %1" : "+s"(d.so) : "s"((I & 3) == 3 ? d.ginc : d.inc) : "scc"); }
template <int I, bool NT> DI void dma_piece(Desc& d) {
  if constexpr ((I & 3) == 0) { dma_m0<I>(d); asm volatile("s_nop 0"); }
  dma_ld<I, NT>(d); dma_adv<I>(d);
}

template <int NB, int KB0, int K> DI void smF(Ctx<NB>& c) { constexpr int kb = KB0 + K / (4 * NB), blk = (K % (4 * NB)) / 4, r = K % 4;
  float x = c.S[blk][kb][r]; fma_ip(x, SCL, c.nm[blk]); c.S[blk][kb][r] = x; }
template <int NB, int KB0, int K> DI void smE(Ctx<NB>& c) { constexpr int kb = KB0 + K / (4 * NB), blk = (K % (4 * NB)) / 4, r = K % 4;
  float x = c.S[blk][kb][r]; exp_ip(x); c.S[blk][kb][r] = x; }
template <int NB, int KB0, int J> DI void smC(Ctx<NB>& c) { constexpr int ko = J / (2 * NB), blk = (J % (2 * NB)) / 2, pr = J % 2;
  c.P[blk][KB0 >> 1][2 * ko + pr] = cvt_v(c.S[blk][KB0 + ko][2 * pr], c.S[blk][KB0 + ko][2 * pr + 1]); }
template <int NB, int KB0, int K> DI void sm_gen(Ctx<NB>& c) {
  constexpr int j = K / 5, pos = K % 5;
  if constexpr (pos < 2) { if constexpr (j < 2 * NB * 2) smF<NB, KB0, 2 * j + pos>(c); }
  else if constexpr (pos < 4) { if constexpr (j < 2 * NB * 2) smE<NB, KB0, 2 * j + pos - 2>(c); }
  else if constexpr (j >= 1 && j - 1 < 2 * NB * 2) smC<NB, KB0, j - 1>(c);
}

DI float max3v(float a, float b, float c) { float r; asm volatile("v_max3_f32 %0, %1, %2, %3" : "=v"(r) : "v"(a), "v"(b), "v"(c)); return r; }
template <int NB, int J> DI void smM(Ctx<NB>& c) { constexpr int kb = J / (2 * NB), hf = (J % (2 * NB)) / NB, blk = J % NB;
  if constexpr (kb == 0 && hf == 0) c.mx[blk] = max3v(c.S[blk][0][0], c.S[blk][0][1], c.S[blk][0][1]);
  else c.mx[blk] = max3v(c.mx[blk], c.S[blk][kb][2 * hf], c.S[blk][kb][2 * hf + 1]); }

template <int NB, bool FIRST, bool NT> DI void phaseA(Ctx<NB>& c, unsigned slot, Desc& d, Prof& pf, int si) {
  PRE(si); wait_bar<24>(); POST(si); RTL(si);
  const unsigned b0 = c.kbase + slot;
  unsigned kb4[4] = {b0, b0 ^ 64u, b0 ^ 128u, b0 ^ 192u};
#define PDA3 4
#define PDA2 3
  constexpr int PD = NB == 3 ? PDA3 : PDA2, RG = PD + 1;
  bf16x8 fr[RG];
  auto rd = [&](auto F) AI { constexpr int f = F, kb = f >> 3, s = f & 7; fr[f % RG] = lds_b128(kb4[s & 3] + 256 * (s >> 2) + 8192 * kb); };
  sfor<PD>(rd);
  dma_m0<0>(d);
  FENCE();
  auto oz = [&](auto Z) AI { constexpr int z = Z;
    if constexpr (z < NB * 64) acc_zero<z>(); };
  auto mf = [&](auto F, auto Bk) AI {
    constexpr int f = F, kb = f >> 3, s = f & 7, blk = Bk;
    if constexpr (blk == 0) { if constexpr (s == 0) mf_vx0<QREG<NB>(s)>(c.S[0][kb], fr[f % RG]); else mf_vx<QREG<NB>(s)>(c.S[0][kb], fr[f % RG]); }
    else                    { if constexpr (s == 0) mf_vv0(c.S[blk][kb], fr[f % RG], c.Q[blk][s]); else mf_vv(c.S[blk][kb], fr[f % RG], c.Q[blk][s]); }
    FENCE();
  };
  auto dsal = [&](auto F) AI { constexpr int f = F;
    if constexpr ((f & 3) == 2) dma_adv<(f >> 2)>(d);
    if constexpr (f == 16) dma_m0<4>(d); };
  if constexpr (NB == 3) {
    if constexpr (!FIRST) {
      sfor<6>([&](auto K) AI { smF<3, 2, K>(c); });
      sfor<4>([&](auto K) AI { smE<3, 2, K>(c); });
      smC<3, 2, 0>(c);
      FENCE();
    }
    sfor<32>([&](auto F) AI {
      constexpr int f = F;
      mf(F, IC<0>{});
      if constexpr (FIRST) { oz(IC<6 * f>{}); oz(IC<6 * f + 1>{}); }
      else if constexpr (f < 20) smE<3, 2, f + 4>(c);
      else if constexpr (f < 23) { smM<3, 6 + 2 * (f - 20)>(c); smM<3, 7 + 2 * (f - 20)>(c); }
      else if constexpr (f >= 26 && f < 29) { smM<3, 12 + 2 * (f - 26)>(c); smM<3, 13 + 2 * (f - 26)>(c); }
      FENCE();
      mf(F, IC<1>{});
      if constexpr (f + PD < 32) rd(IC<f + PD>{});
      FENCE();
      if constexpr (FIRST) { oz(IC<6 * f + 2>{}); oz(IC<6 * f + 3>{}); }
      else if constexpr (f + 6 < 24) smF<3, 2, f + 6>(c);
      FENCE();
      mf(F, IC<2>{});
      if constexpr ((f & 3) == 1) dma_ld<(f >> 2), NT>(d);
      if constexpr (FIRST) { oz(IC<6 * f + 4>{}); oz(IC<6 * f + 5>{}); }
      else if constexpr (!(f & 1) && f / 2 + 1 < 12) smC<3, 2, f / 2 + 1>(c);
      else if constexpr (f == 11 || f == 15 || f == 19) { smM<3, 2 * ((f - 11) / 4)>(c); smM<3, 2 * ((f - 11) / 4) + 1>(c); }
      dsal(F);
      FENCE();
    });
  } else {
    constexpr int NOPS = NB * 4 * 5 + 5, GF = 22 * NB;
    sfor<32>([&](auto F) AI {
      constexpr int f = F;
      sfor<NB>([&](auto Bk) AI {
        constexpr int blk = Bk, g = f * NB + blk;
        mf(F, Bk);
        if constexpr (blk == 0 && f + PD < 32) rd(IC<f + PD>{});
        if constexpr (blk == 1 && (f & 3) == 1) dma_ld<(f >> 2), NT>(d);
        FENCE();
        if constexpr (FIRST) { oz(IC<2 * g>{}); oz(IC<2 * g + 1>{}); }
        else {
          constexpr int lo = (g < GF) ? (g * NOPS) / GF : NOPS, hi = (g + 1 < GF) ? ((g + 1) * NOPS) / GF : NOPS;
          sfor<hi - lo>([&](auto J) AI { sm_gen<NB, 2, lo + J>(c); });
          if constexpr (g >= GF && g < GF + 4 * NB) smM<NB, g - GF>(c);
          if constexpr (g >= 26 * NB && g < 28 * NB) smM<NB, 4 * NB + g - 26 * NB>(c);
        }
        if constexpr (blk == 1) dsal(F);
        FENCE();
      });
    });
  }
}

template <int NB> DI float rowmax16(const f32x4* s) {
  float m = max3f(s[0][0], s[0][1], s[0][2]);
  m = max3f(m, s[0][3], s[1][0]); m = max3f(m, s[1][1], s[1][2]); m = max3f(m, s[1][3], s[2][0]);
  m = max3f(m, s[2][1], s[2][2]); m = max3f(m, s[2][3], s[3][0]); m = max3f(m, s[3][1], s[3][2]);
  return max2f(m, s[3][3]);
}
DI float xl32(float v) { auto p = __builtin_amdgcn_permlane32_swap(__float_as_uint(v), __float_as_uint(v), false, false); return max2f(__uint_as_float(p[0]), __uint_as_float(p[1])); }
DI float xl16(float v) { auto p = __builtin_amdgcn_permlane16_swap(__float_as_uint(v), __float_as_uint(v), false, false); return max2f(__uint_as_float(p[0]), __uint_as_float(p[1])); }
constexpr float THR_RAW = THR / SCL;

template <int NB> DI void phaseB0(Ctx<NB>& c) {
  asm volatile("s_nop 7" ::: "memory");
  FENCE();
  sfor<NB>([&](auto Bk) AI { constexpr int blk = Bk; c.mx[blk] = rowmax16<NB>(c.S[blk]); });
  sfor<NB>([&](auto Bk) AI { constexpr int blk = Bk; const float m = xl16(xl32(c.mx[blk])); c.nm[blk] = -SCL * m; c.thr[blk] = m + THR_RAW; c.dl[blk] = 0.f;
    c.L[blk] = f32x4{0.f, 0.f, 0.f, 0.f}; });
  sfor<NB * 20 + 5>([&](auto J) AI { sm_gen<NB, 0, J>(c); });
}

#define BDMA 1
template <int NB, bool FINAL, bool NT> DI void phaseB(Ctx<NB>& c, unsigned slot, Desc& d, Prof& pf, int si) {
  PRE(si); wait_bar<24>(); POST(si); RTL(si);
  const unsigned b0 = c.vbase + slot;
#define PDB3 4
#define PDB2 3
  constexpr int PD = NB == 3 ? PDB3 : PDB2, RG = PD + 1;
  bf16x4 flo[RG], fhi[RG];
  unsigned vb[8];
  auto rdl = [&](auto F) AI { constexpr int f = F, u = f >> 4, j = (f >> 1) & 7, hi = f & 1; flo[f % RG] = lds_tr(vb[j] + 256 * hi + 16384 * u); };
  auto rdh = [&](auto F) AI { constexpr int f = F, u = f >> 4, j = (f >> 1) & 7, hi = f & 1; fhi[f % RG] = lds_tr(vb[j] + 256 * hi + 16384 * u + 8192); };
  vb[0] = b0; vb[1] = b0 ^ 32u; vb[2] = b0 ^ 64u; vb[3] = b0 ^ 96u;
  sfor<PD>([&](auto F) AI { rdl(F); rdh(F); });
  asm volatile("" ::: "memory");
  FENCE();
  dma_m0<0>(d);
  sfor<6>([&](auto J) AI { if constexpr (2 * (2 + J) >= PD) { vb[2 + J] = b0 ^ (32u * (2 + J)); tie_v(vb[2 + J]); } });
  unsigned long long chg = 0;
  if constexpr (!FINAL) {
    sfor<2 * NB>([&](auto J) AI { smM<NB, 6 * NB + J>(c); });
    sfor<NB>([&](auto Bk) AI { constexpr int blk = Bk; chg |= __ballot(c.mx[blk] > c.thr[blk]); });
    if (__builtin_expect(chg != 0ull, 0)) {
      sfor<NB>([&](auto Bk) AI { constexpr int blk = Bk;
        const float m = xl16(xl32(c.mx[blk]));
        const float t = fma_s(m, SCL, c.nm[blk]);
        const bool mv = m > c.thr[blk];
        c.dl[blk] = mv ? t : 0.f;
        c.nm[blk] -= c.dl[blk];
        c.thr[blk] = mv ? m + THR_RAW : c.thr[blk]; });
    }
  }
  FENCE();
  auto mfl = [&](auto U, auto Bk) AI { mf_vv(c.L[Bk], c.ones, __builtin_bit_cast(bf16x8, c.P[Bk][U])); FENCE(); };
  auto mfo = [&](auto F, auto Bk) AI { constexpr int f = F, u = f >> 4, db = ((f >> 1) & 7) + 8 * (f & 1);
    mf_o<OREG(Bk, db)>(cat8(flo[f % RG], fhi[f % RG]), __builtin_bit_cast(bf16x8, c.P[Bk][u])); FENCE(); };
  auto dsal = [&](auto F) AI { constexpr int f = F;
    if constexpr ((f & 3) == 2) dma_adv<(f >> 2)>(d);
    if constexpr (f == 16) dma_m0<4>(d); };
  if constexpr (NB == 3 && BDMA == 0) {
    if constexpr (FINAL) {
      sfor<20>([&](auto K) AI { smF<3, 2, K>(c); });
      sfor<9>([&](auto K) AI { smE<3, 2, K>(c); });
      FENCE();
    }
    sfor<2>([&](auto U_) AI {
      constexpr int u = U_;
      sfor<3>([&](auto Bk) AI {
        constexpr int blk = Bk;
        mfl(U_, Bk);
        if constexpr (!FINAL) {
          if constexpr (u == 0) { smF<3, 0, 2 * blk>(c); smF<3, 0, 2 * blk + 1>(c); } else smE<3, 0, 12 + blk>(c);
        } else if constexpr (u == 0) smE<3, 2, 9 + blk>(c);
        FENCE();
      });
      sfor<16>([&](auto FF) AI {
        constexpr int f = 16 * u + FF, nd = f - ((f + 2) >> 2);
        mfo(IC<f>{}, IC<0>{});
        if constexpr ((f & 3) == 1) dma_ld<(f >> 2), NT>(d);
        else if constexpr (!FINAL) { constexpr int e = u == 0 ? nd : nd + 3; if constexpr (e < 24) smE<3, 0, e>(c); }
        else if constexpr (u == 0) smE<3, 2, 12 + nd>(c);
        FENCE();
        mfo(IC<f>{}, IC<1>{});
        if constexpr (f + PD < 32) rdl(IC<f + PD>{});
        FENCE();
        if constexpr (!FINAL) {
          if constexpr (f < 16) smF<3, 0, f + 6>(c);
          else if constexpr (f < 22) smC<3, 0, f - 16>(c);
          else if constexpr (f < 24) smF<3, 0, f>(c);
          else if constexpr (f < 30) smC<3, 0, f - 18>(c);
        } else {
          if constexpr (f < 4) smF<3, 2, 20 + f>(c);
          else if constexpr (f < 16) smC<3, 2, f - 4>(c);
        }
        FENCE();
        mfo(IC<f>{}, IC<2>{});
        if constexpr (f + PD < 32) rdh(IC<f + PD>{});
        dsal(IC<f>{});
        FENCE();
      });
    });
  } else if constexpr (NB == 3) {
    if constexpr (FINAL) {
      sfor<20>([&](auto K) AI { smF<3, 2, K>(c); });
      sfor<9>([&](auto K) AI { smE<3, 2, K>(c); });
      FENCE();
    }
    sfor<2>([&](auto U_) AI {
      constexpr int u = U_;
      sfor<3>([&](auto Bk) AI {
        constexpr int blk = Bk;
        mfl(U_, Bk);
        if constexpr (!FINAL) {
          if constexpr (u == 0) { smF<3, 0, 2 * blk>(c); smF<3, 0, 2 * blk + 1>(c); }
          else if constexpr (blk < 2) smE<3, 0, 16 + blk>(c);
          else { smF<3, 0, 18>(c); smF<3, 0, 19>(c); }
        } else if constexpr (u == 0) smE<3, 2, 9 + blk>(c);
        FENCE();
      });
      sfor<16>([&](auto FF) AI {
        constexpr int f = 16 * u + FF, nd = f - ((f + 2) >> 2);
        constexpr bool dm = (f & 3) == 1;
        mfo(IC<f>{}, IC<0>{});
        if constexpr (!FINAL) {
          if constexpr (f < 16) smE<3, 0, f>(c);
          else if constexpr (f == 16) { smF<3, 0, 20>(c); smF<3, 0, 21>(c); }
          else if constexpr (f == 17) { smF<3, 0, 22>(c); smF<3, 0, 23>(c); }
          else if constexpr (f < 24) smE<3, 0, f>(c);
          else if constexpr (f == 24) { smC<3, 0, 6>(c); smC<3, 0, 7>(c); }
          else if constexpr (f == 25) { smC<3, 0, 9>(c); smC<3, 0, 10>(c); }
          else if constexpr (f == 26) smC<3, 0, 11>(c);
        } else {
          if constexpr (f < 12) smE<3, 2, 12 + f>(c);
          else if constexpr (f == 12) { smC<3, 2, 5>(c); smC<3, 2, 6>(c); }
          else if constexpr (f == 13) { smC<3, 2, 8>(c); smC<3, 2, 9>(c); }
          else if constexpr (f == 14) { smC<3, 2, 10>(c); smC<3, 2, 11>(c); }
        }
        FENCE();
        mfo(IC<f>{}, IC<1>{});
        if constexpr (f + PD < 32) { rdl(IC<f + PD>{}); if constexpr (dm) rdh(IC<f + PD>{}); }
        FENCE();
        if constexpr (!dm) {
          if constexpr (!FINAL) {
            if constexpr (f < 16) smF<3, 0, 6 + nd>(c);
            else if constexpr (f == 16) smC<3, 0, 0>(c);
            else if constexpr (f >= 18 && f <= 20) smC<3, 0, f - 17>(c);
            else if constexpr (f == 22 || f == 23) smC<3, 0, f - 18>(c);
            else if constexpr (f == 24) smC<3, 0, 8>(c);
          } else {
            if constexpr (f < 5) smF<3, 2, 20 + nd>(c);
            else if constexpr (f >= 6 && f <= 8) smC<3, 2, f - 6>(c);
            else if constexpr (f == 10 || f == 11) smC<3, 2, f - 7>(c);
            else if constexpr (f == 12) smC<3, 2, 7>(c);
          }
        }
        FENCE();
        mfo(IC<f>{}, IC<2>{});
        if constexpr (dm) dma_ld<(f >> 2), NT>(d);
        else if constexpr (f + PD < 32) rdh(IC<f + PD>{});
        dsal(IC<f>{});
        FENCE();
      });
    });
  } else {
    constexpr int NU = NB * 4, GH = 17 * NB;
    sfor<2>([&](auto U_) AI {
      constexpr int u = U_;
      sfor<17>([&](auto FF) AI {
        constexpr int ff = FF, f = 16 * u + ff - 1;
        sfor<NB>([&](auto Bk) AI {
          constexpr int blk = Bk, g = u * GH + ff * NB + blk;
          if constexpr (ff == 0) mfl(U_, Bk);
          else {
            mfo(IC<f>{}, Bk);
            if constexpr (f + PD < 32) { if constexpr (blk == 0) rdl(IC<f + PD>{}); else rdh(IC<f + PD>{}); }
            if constexpr (blk == 1 && (f & 3) == 1) dma_ld<(f >> 2), NT>(d);
            if constexpr (blk == 0) dsal(IC<f>{});
            FENCE();
          }
          if constexpr (FINAL) {
            constexpr int NOPS = NU * 5 + 5, GF = GH - 2;
            constexpr int lo = (g < GF) ? (g * NOPS) / GF : NOPS, hi = (g + 1 < GF) ? ((g + 1) * NOPS) / GF : NOPS;
            sfor<hi - lo>([&](auto J) AI { sm_gen<NB, 2, lo + J>(c); });
          } else {
            constexpr int G1 = 2 * GH - 4, NOPS = NU * 4;
            constexpr int lo = g < G1 ? (g * NOPS) / G1 : NOPS, hi = g + 1 < G1 ? ((g + 1) * NOPS) / G1 : NOPS;
            sfor<hi - lo>([&](auto J) AI { constexpr int k = lo + J; sm_gen<NB, 0, (k / 4) * 5 + (k % 4)>(c); });
            sfor<NU>([&](auto UU) AI {
              constexpr int un = UU;
              constexpr int gdone = ((4 * un + 4) * G1 + NOPS - 1) / NOPS + 1, gmin = GH + NB + un;
              constexpr int gat = gdone > gmin ? gdone : gmin;
              if constexpr (g == (gat < 2 * GH - 1 ? gat : 2 * GH - 1)) smC<NB, 0, un>(c);
            });
          }
          FENCE();
        });
      });
    });
  }
  if constexpr (!FINAL) {
    if (__builtin_expect(chg != 0ull, 0)) {
      asm volatile("s_nop 7" ::: "memory");
      FENCE();
      sfor<NB>([&](auto Bk) AI { constexpr int blk = Bk;
        const float f = __builtin_amdgcn_exp2f(-c.dl[blk]);
        c.L[blk] *= f;
        sfor<64>([&](auto Z) AI { acc_mul<OREG(blk, 0) + Z>(f); }); });
      FENCE();
      asm volatile("s_nop 3" ::: "memory");
    }
  }
}

template <int NB> struct Epi {
  bf16x8 of[NB][8];
  f32x4  y[2][NB][2];
  float  rl[NB];
  unsigned vP[NB];
  u32x4  srdP;
};
template <int NB, int BUF, int K> DI void drain_op(Epi<NB>& e, u32x4 (&t)[2], unsigned so) {
  constexpr int blk = K / 13, pos = K % 13;
  if constexpr (pos < 8) { float v = e.y[BUF][blk][pos >> 2][pos & 3]; e.y[BUF][blk][pos >> 2][pos & 3] = mul_v(v, e.rl[blk]); }
  else if constexpr (pos < 12) { constexpr int q = pos - 8; t[blk & 1][q] = cvt_v(e.y[BUF][blk][q >> 1][2 * (q & 1)], e.y[BUF][blk][q >> 1][2 * (q & 1) + 1]); }
  else st16_sc1(t[blk & 1], e.vP[blk], so, e.srdP);
}
template <int NB, int F> DI void yrd(bf16x8 (&fr)[4], unsigned b0) {
  constexpr int nb = F >> 3, s = F & 7; const unsigned a = (b0 ^ (32u * nb)) + 4096 * s; fr[F & 3] = lds_tr2(a, a + 2048);
}
template <int NB, int HF> DI void halfY(Epi<NB>& e, bf16x8 (&fr)[4], unsigned b0, unsigned so) {
  u32x4 t[2];
  constexpr int NOPS = NB * 13, G0 = 2, G1 = 16 * NB - 1;
  FENCE();
  sfor<16>([&](auto F) AI {
    constexpr int fl = F, f = 16 * HF + fl, nb = fl >> 3, s = f & 7;
    sfor<NB>([&](auto Bk) AI {
      constexpr int blk = Bk, g = fl * NB + blk;
      if constexpr (s == 0) mf_vv0(e.y[HF][blk][nb], fr[f & 3], e.of[blk][s]); else mf_vv(e.y[HF][blk][nb], fr[f & 3], e.of[blk][s]);
      if constexpr (blk == 0 && f + 3 < 32) yrd<NB, f + 3>(fr, b0);
      constexpr int lo = g < G0 ? 0 : (g >= G1 ? NOPS : ((g - G0) * NOPS) / (G1 - G0)), hi = g + 1 < G0 ? 0 : (g + 1 >= G1 ? NOPS : ((g + 1 - G0) * NOPS) / (G1 - G0));
      sfor<hi - lo>([&](auto J) AI { drain_op<NB, HF ^ 1, lo + J>(e, t, so); });
      FENCE();
    });
  });
}

struct Red {
  u32x4 srdF, srdR, srdO;
  unsigned tlo, thi;
  unsigned vF;
  unsigned vR, vO;
  unsigned lane;
};
DI void flag_set(const Red& r, unsigned line) {
  u32x2 tg = {r.tlo, r.thi};
  st8_sc1(tg, r.vF, line * 128u, r.srdF);
}
DI void poll_issue(const Red& r, unsigned line) {
  const unsigned vo = r.lane < 16u ? line * 128u + r.lane * 8u : OOB;
  asm volatile("buffer_load_dwordx2 a[126:127], %0, %1, 0 offen sc1" :: "v"(vo), "s"(r.srdF) : "memory", XCLOB);
}
DI bool poll_ok(const Red& r) {
  unsigned lo, hi;
  asm volatile("v_accvgpr_read_b32 %0, a126\n\tv_accvgpr_read_b32 %1, a127" : "=v"(lo), "=v"(hi) :: XCLOB);
  return __ballot(r.lane >= 16u || (lo == r.tlo && hi == r.thi)) == ~0ull;
}
DI void flag_wait(const Red& r, unsigned line) {
#pragma unroll 1
  for (int it = 0; it < (1 << 22); ++it) {
    poll_issue(r, line);
    wait_vm<0>();
    if (poll_ok(r)) break;
    __builtin_amdgcn_s_sleep(1);
  }
}
template <int R, int OFF> DI void pa_load(unsigned vo, unsigned so, const u32x4& srd) {
  asm volatile("buffer_load_dwordx4 a[%c0:%c1], %2, %3, %4 offen offset:%5 sc1" :: "i"(R), "i"(R + 3), "v"(vo), "s"(srd), "s"(so), "i"(OFF) : XCLOB); }
template <int R> DI void red_issue(const Red& r, unsigned c, unsigned idx) {
  const unsigned fr = (idx >> 1) * 14u + 2u * c + (idx & 1u);
  const unsigned so = rfl(fr * 1024u);
  sfor<8>([&](auto J) AI {
    const unsigned s2 = so + 2u * J * PARTB;
    pa_load<R + 8 * J, 0>(r.vR, s2, r.srdR);
    pa_load<R + 8 * J + 4, 256>(r.vR, s2, r.srdR);
  });
}
template <int R> DI void red_finish(const Red& r, const bf16x8& sel, unsigned c, unsigned idx) {
  f32x4 a0, a1;
  asm volatile("" ::: XCLOB);
  sfor<8>([&](auto J) AI {
    if constexpr (J == 0) { mf_vx0<R>(a0, sel); mf_vx0<R + 4>(a1, sel); }
    else { mf_vx<R + 8 * J>(a0, sel); mf_vx<R + 8 * J + 4>(a1, sel); }
  });
  asm volatile("s_nop 7\n\ts_nop 1" ::: "memory", XCLOB);
  FENCE();
  u32x4 t = {cvt_v(a0[0], a0[1]), cvt_v(a0[2], a0[3]), cvt_v(a1[0], a1[1]), cvt_v(a1[2], a1[3])};
  const unsigned so = rfl((idx >> 1) * (16u * 2u * DM * 2u) + (2u * c + (idx & 1u)) * 64u);
  st16(t, r.vO, so, r.srdO);
}

template <int NB>
DI void body(const __hip_bfloat16* __restrict__ x, const __hip_bfloat16* __restrict__ kc, const __hip_bfloat16* __restrict__ vc,
             const __hip_bfloat16* __restrict__ wq, const __hip_bfloat16* __restrict__ wo, char* __restrict__ ws, __hip_bfloat16* __restrict__ out,
             int br, int b, int h, unsigned jx, unsigned w, unsigned lane, unsigned lds0, uint64_t pc0) {
  const int grp = br * 8 + b;
  const unsigned g = lane >> 4, cc = lane & 15u, q4 = cc >> 2, p4 = lane & 3u, hi5 = lane >> 5, ch = lane & 31u;
  Ctx<NB> c;
  const u32x4 srdK = mk_srd((const char*)kc + ((size_t)(b * NH + h) * SK) * (DH * 2), SK * DH * 2);
  const u32x4 srdV = mk_srd((const char*)vc + ((size_t)(b * NH + h) * SK) * (DH * 2), SK * DH * 2);
  const u32x4 srdWq = mk_srd((const char*)wq + h * (DH * 2), DM * NH * DH * 2 - h * (DH * 2));
  const u32x4 srdWo = mk_srd((const char*)wo + (size_t)h * DH * DM * 2, DH * DM * 2);
  const u32x4 srdX = mk_srd((const char*)x + (size_t)b * SQ * DM * 2, SQ * DM * 2);
  const u32x4 srdF = mk_srd(ws + FLAG_OFF + (size_t)grp * 4096, 4096);
  const unsigned rb[3] = {2u * w, 2u * w + 1u, 8u + w};
  Prof pf;
  POST(0); RT(0);

  unsigned dummy = 0;
  u32x2 ep;
  ld8_sc1(ep, 28u * 128u, srdF);
  const unsigned vWq0 = 8192u * hi5 + 16u * (ch ^ (2u * (2u * (w & 1u) + hi5))), vWq1 = vWq0 ^ (16u * 8u);
  const unsigned mbw = rfl(lds0 + 1024u * (w & 1u));
  unsigned vx[NB];
  sfor<NB>([&](auto Bk) AI { vx[Bk] = (16u * rb[Bk] + (lane >> 2)) * (DM * 2) + 16u * (p4 ^ (2u * hi5)); });
  auto xld = [&](auto K_) AI { constexpr int k = K_, ks = k / NB, bk = k % NB; xa_load<XREG<NB>(ks, bk), 64 * ks>(vx[bk], srdX); };
  if constexpr (NB == 2) {
    unsigned so = rfl(2u * (w & 1u) * 8192u);
    sfor<16>([&](auto I) AI { constexpr int i = I; so = dma<SLOT * 1 + 2048 * i, false>(mbw, so, 4u * 8192u, ((i >> 1) & 1) ? vWq1 : vWq0, srdWq); });
  }
  POST(53);
  sfor<6 * NB>(xld);
  POST(54);
  if constexpr (NB == 2) {
    unsigned so = rfl((64u + 2u * (w & 1u)) * 8192u);
    sfor<16>([&](auto I) AI { constexpr int i = I; so = dma<SLOT * 0 + 2048 * i, false>(mbw, so, 4u * 8192u, ((i >> 1) & 1) ? vWq1 : vWq0, srdWq); });
  } else {
    if (w == 0) {
      sfor<3>([&](auto I) AI {
        const unsigned idx = 64u * I + lane;
        const unsigned vo = (64u * (2u + (idx >> 5)) + 8u * b + ((idx >> 2) & 7u)) * 8192u + (idx & 3u) * 128u;
        pf_load(dummy, idx < 160u ? vo : OOB, srdWq);
      });
    } else {
      const u32x4 srdC = mk_srd((const void*)(pc0 & ~127ull), CODE_PF);
      pf_load(dummy, lane < CODE_PF / 4096u ? (jx * (CODE_PF / 4096u) + lane) * 128u : OOB, srdC);
    }
  }

  PRE(1);
  c.kbase = lds0 + 512u * cc + 16u * (g ^ (cc & 3u)) + 64u * (cc >> 2);
  c.vbase = lds0 + 512u * (4u * g + q4) + 32u * (4u * (g & 1u) + q4) + 8u * p4;
  { u32x4 o = {0x3F803F80u, 0x3F803F80u, 0x3F803F80u, 0x3F803F80u}; c.ones = __builtin_bit_cast(bf16x8, o); tie_v8(c.ones); }
  const unsigned mb4 = rfl(lds0 + 4096u * w);
  Desc dA, dB;
  dA.srd = srdK; dB.srd = srdV; dA.inc = dB.inc = 0u; dA.ginc = dB.ginc = 16384u;
  sfor<4>([&](auto K) AI { dA.v[K] = 512u * hi5 + 16u * (ch ^ (8u * (w & 1u) + 2u * K + hi5)); dB.v[K] = 512u * hi5 + 16u * (ch ^ (2u * (2u * K + hi5))); });

  unsigned tlo, thi;
  {
    f32x4 qa[NB][16];
    u32x4 qt[NB][8];
    const unsigned wbase = lds0 + 512u * (8u * g + q4) + 32u * (q4 + 4u * (g & 1u)) + 8u * p4;
    const unsigned pa = 4u * (4u * cc + (g ^ (2u * (cc >> 3))));
    unsigned vkv[4];
    auto qcv = [&](auto BB, auto SP, auto K_) AI {
      constexpr int bb = BB, sp = SP, k = K_;
      if constexpr (k < 4) qt[bb][sp][k] = cvt_v(qa[bb][2 * sp + (k >> 1)][2 * (k & 1)], qa[bb][2 * sp + (k >> 1)][2 * (k & 1) + 1]);
      else { unsigned x0 = qt[bb][sp][k & 1], x1 = qt[bb][sp][2 + (k & 1)];
        if constexpr (k < 6) swap32(x0, x1); else swap16(x0, x1);
        qt[bb][sp][k & 1] = x0; qt[bb][sp][2 + (k & 1)] = x1; }
    };
    sfor<7>([&](auto C_) AI {
      constexpr int C = C_;
      PRE(2 + C);
      if constexpr (NB == 3) { constexpr int n[7] = {6, 12, 18, 28, 38, 48, 63}; wait_vm<n[C]>(); }
      else { constexpr int n[7] = {20, 8, 8, 0, 0, 0, 0}; wait_vm<n[C]>(); }
      if constexpr (C == 0) {
        tie_v2(ep);
        tlo = rfl(ep[0]) + 1u; thi = rfl(ep[1]) + (tlo == 0u ? 1u : 0u);
        sfor<8 * NB>([&](auto K) AI { xa_perm<XREG<NB>((int)K / (4 * NB), 0) + (int)K % (4 * NB)>(pa); });
      }
      if constexpr (C == 2) tie_v(dummy);
      asm volatile("s_waitcnt lgkmcnt(0)\n\ts_barrier" ::: "memory", XCLOB);
      POST(2 + C); RT(2 + C);
      const unsigned b0 = wbase + SLOT * ((C + 1) & 1);
      constexpr int PD = 5;
      bf16x8 fr[PD + 1];
      auto rd = [&](auto F) AI { constexpr int f = F, be = f >> 1, u = f & 1;
        const unsigned a = (b0 ^ (32u * (be & 7))) + 256 * (be >> 3) + 16384 * u; fr[f % (PD + 1)] = lds_tr2(a, a + 2048); };
      sfor<PD>(rd);
      unsigned so = 0;
      if constexpr (NB == 2 && C >= 1 && C <= 5) so = rfl((64u * (C + 1) + 2u * (w & 1u)) * 8192u);
      if constexpr (NB == 3 && C >= 2 && C <= 4) {
        so = rfl((C == 3 ? SLOT : 0u) + 1024u * w);
        if constexpr (C == 2) sfor<4>([&](auto I) AI { vkv[I] = 512u * hi5 + 16u * (ch ^ hi5 ^ ((4u * I + 2u * w) & 15u)); });
        if constexpr (C == 4) sfor<2>([&](auto I) AI { vkv[I] = 512u * hi5 + 16u * (ch ^ (2u * ((2u * (2u * I + w) + hi5) & 7u))); });
      }
      if constexpr (C == 6) { dA.mb = mb4; dA.so = 2u * SLOT + 4096u * w; dma_m0<0>(dA); }
      FENCE();
      sfor<32>([&](auto F) AI {
        constexpr int f = F, be = f >> 1, u = f & 1;
        sfor<NB>([&](auto Bk) AI {
          constexpr int blk = Bk, gp = f * NB + blk;
          constexpr int XR = XREG<NB>(2 * C + u, blk);
          if constexpr (C == 0 && u == 0) mf_vx0<XR>(qa[blk][be], fr[f % (PD + 1)]); else mf_vx<XR>(qa[blk][be], fr[f % (PD + 1)]);
          if constexpr (blk == 0 && f + PD < 32) rd(IC<f + PD>{});
          if constexpr (NB == 2 && C >= 1 && C <= 5 && gp < 16) so = dma<SLOT * (C & 1) + 2048 * gp, false>(mbw, so, 4u * 8192u, ((gp >> 1) & 1) ? vWq1 : vWq0, srdWq);
          if constexpr (C <= 1 && NB == 3 && (gp & 7) == 1 && (gp >> 3) < 4 * NB) xld(IC<(6 + 4 * C) * NB + (gp >> 3)>{});
          if constexpr (C <= 1 && NB == 2 && gp >= 17 && ((gp - 17) & 3) == 0 && ((gp - 17) >> 2) < 4 * NB) xld(IC<(6 + 4 * C) * NB + ((gp - 17) >> 2)>{});
          if constexpr (NB == 3 && C >= 2 && C <= 4 && blk == 1 && (f & 1) == 0) {
            constexpr int i = f >> 1;
            if constexpr (C < 4) so = dma<SLOT * C + 2048 * i, true>(mbw, so, 2048u, vkv[i & 3], srdK);
            else                 so = dma<SLOT * 4 + 2048 * i, true>(mbw, so, 2048u, vkv[i & 1], srdV);
          }
          if constexpr (C == 6 && blk == 1 && (f & 3) == 1) dma_ld<(f >> 2), true>(dA);
          if constexpr (C == 6 && blk == 1 && (f & 3) == 2) dma_adv<(f >> 2)>(dA);
          if constexpr (C == 6 && blk == 1 && f == 16) dma_m0<4>(dA);
          if constexpr (C <= 5 && (gp & 3) == 2 && (gp >> 2) < 8 * NB) xa_perm<XREG<NB>(2 * C + 2 + (gp >> 2) / (4 * NB), 0) + (gp >> 2) % (4 * NB)>(pa);
          if constexpr (C == 6 && gp >= 8 * NB) {
            constexpr int k = gp - 8 * NB, un = k >> 2, q = k & 3, sp2 = un / NB, bb = un % NB;
            qcv(IC<bb>{}, IC<sp2>{}, IC<2 * q>{}); qcv(IC<bb>{}, IC<sp2>{}, IC<2 * q + 1>{});
          }
          FENCE();
        });
      });
    });
    PRE(9);
    asm volatile("s_nop 7" ::: "memory", XCLOB);
    FENCE();
    sfor<4>([&](auto Q_) AI { sfor<2 * NB>([&](auto U_) AI { constexpr int q = Q_, un = 6 * NB + U_, sp2 = un / NB, bb = un % NB;
        qcv(IC<bb>{}, IC<sp2>{}, IC<2 * q>{}); qcv(IC<bb>{}, IC<sp2>{}, IC<2 * q + 1>{}); }); });
    sfor<NB - 1>([&](auto Bk) AI { sfor<8>([&](auto Sp) AI { c.Q[Bk + 1][Sp] = __builtin_bit_cast(bf16x8, qt[Bk + 1][Sp]); }); });
    sfor<32>([&](auto K) AI { acc_wr<QREG<NB>(0) + K>(qt[0][K / 4][K % 4]); });
    FENCE();
    asm volatile("s_nop 1" ::: "memory");
  }

  {
    dB.mb = mb4 + SLOT; dB.so = SLOT + 4096u * w;
    phaseA<NB, true, true>(c, 2u * SLOT, dB, pf, 10);
    PRE(11);
    phaseB0<NB>(c);
  }
  unsigned sl = 3u * SLOT, pv = 2u * SLOT;
  unsigned soA = 3u * SLOT + 4096u * w, soB = 2u * SLOT + 4096u * w, stepB = SLOT;
  for (int t = 1; t < 16; ++t) {
    if (t >= 14) {
      dA.srd = dB.srd; dA.inc = dB.inc; dA.ginc = dB.ginc; soA = soB - (t == 15 ? 128u : 0u);
      sfor<4>([&](auto K) AI { dA.v[K] = mov_v(dB.v[K]); });
    }
    dA.mb = mb4 + pv; dA.so = soA; soA += SLOT;
    phaseA<NB, false, true>(c, sl, dA, pf, 10 + 2 * t);
    pv = sl; sl = sl == 4u * SLOT ? 0u : sl + SLOT;
    if (t == 14) {
      dB.srd = srdWo; dB.inc = 6144u; dB.ginc = 96256u; soB = 28672u * w; stepB = 256u;
      const unsigned l = lane_id(), vWo = 896u * (l >> 3) + 16u * ((l & 7u) ^ (2u * ((l >> 4) & 3u)));
      sfor<4>([&](auto K) AI { dB.v[K] = mov_v(vWo); });
    }
    dB.mb = mb4 + pv; dB.so = soB; soB += stepB;
    phaseB<NB, false, true>(c, sl, dB, pf, 11 + 2 * t);
    pv = sl; sl = sl == 4u * SLOT ? 0u : sl + SLOT;
  }
  dB.mb = mb4 + pv; dB.so = soB - 128u;
  phaseB<NB, true, true>(c, sl, dB, pf, 42);
  PRE(43);
  pv = sl; sl = sl == 4u * SLOT ? 0u : sl + SLOT;

  asm volatile("s_nop 7" ::: "memory");
  FENCE();
  Epi<NB> e;
  sfor<NB>([&](auto Bk) AI { constexpr int blk = Bk;
    tie_v4(c.L[blk]);
    e.rl[blk] = __builtin_amdgcn_rcpf(c.L[blk][0]);
    sfor<8>([&](auto Sp) AI { constexpr int sp = Sp;
      constexpr int R = OREG(blk, 2 * sp);
      const float a0 = acc_rd<R>(), a1 = acc_rd<R + 1>(), a2 = acc_rd<R + 2>(), a3 = acc_rd<R + 3>();
      const float a4 = acc_rd<R + 4>(), a5 = acc_rd<R + 5>(), a6 = acc_rd<R + 6>(), a7 = acc_rd<R + 7>();
      u32x4 t = {cvt_v(a0, a1), cvt_v(a2, a3), cvt_v(a4, a5), cvt_v(a6, a7)};
      e.of[blk][sp] = __builtin_bit_cast(bf16x8, t); });
    sfor<2>([&](auto Nb) AI { e.y[1][blk][Nb] = f32x4{0.f, 0.f, 0.f, 0.f}; tie_v4(e.y[1][blk][Nb]); }); });
  FENCE();
  asm volatile("s_nop 1" ::: "memory");
  POST(43);
  const unsigned laneE = lane_id(), gE = laneE >> 4, ccE = laneE & 15u, q4E = ccE >> 2;
  const unsigned obase = lds0 + 128u * (4u * gE + q4E) + 32u * ((2u * gE + (q4E >> 1)) & 3u) + 8u * (laneE & 3u);
  e.srdP = mk_srd(ws + (size_t)(grp * NH + h) * PARTB, PARTB);
  sfor<NB>([&](auto Bk) AI { e.vP[Bk] = rb[Bk] * (14u * 1024u) + laneE * 16u; });
  Red r;
  r.srdF = srdF; r.tlo = tlo; r.thi = thi; r.lane = laneE;
  r.vF = laneE == 0u ? (unsigned)h * 8u : OOB;
  r.srdR = mk_srd(ws + (size_t)grp * NH * PARTB, NH * PARTB);
  r.srdO = mk_srd((char*)out + ((size_t)b * SQ * 2 * DM + br * DM) * 2, (SQ * 2 * DM - br * DM) * 2);
  r.vR = (gE & 1u) * PARTB + (32u * (gE >> 1) + ccE) * 16u;
  r.vO = ccE * (2u * DM * 2u) + 32u * (gE & 1u) + 16u * (gE >> 1);

  asm volatile("; EPI_BEGIN" ::: XCLOB);
  u32x4 sv; sfor<4>([&](auto I) AI { constexpr int i = I; const unsigned m7 = ccE & 7u; const bool on = (gE >> 1) == (ccE >> 3);
    sv[i] = (on && m7 == 2u * i) ? 0x00003F80u : ((on && m7 == 2u * i + 1u) ? 0x3F800000u : 0u); });
  bf16x8 sel = __builtin_bit_cast(bf16x8, sv); tie_v8(sel);
  const unsigned c0 = (unsigned)h >> 2, ib = 16u + ((unsigned)h & 3u);
  const unsigned iw = w == 2u ? (unsigned)h : ib;
  const unsigned lw = w == 2u ? c0 : (ib >> 1) - 8u;
  PRE(44); wait_bar<24>(); POST(44); RT(44);
#pragma unroll 1
  for (int p = 0; p < 7; ++p) {
    const unsigned b0 = obase + sl;
    bf16x8 fr[4];
    yrd<NB, 0>(fr, b0); yrd<NB, 1>(fr, b0); yrd<NB, 2>(fr, b0);
    halfY<NB, 0>(e, fr, b0, p == 0 ? OOB : (unsigned)(2 * p - 1) * 1024u);
    wait_vm<2 * NB>();
    if (p >= 2) flag_set(r, (unsigned)(p - 2) * 4u + w);
    if (p <= 2) { dB.mb = mb4 + pv; dB.so = 28672u * w + 128u * (unsigned)(4 + p); sfor<8>([&](auto I) AI { dma_piece<I, false>(dB); }); }
    if constexpr (NB == 2) {
      if (p >= 3) {
        const unsigned cf = p - 5, cl = p - 4, cp = p - 3;
        if (p >= 5 && (w == 2u || c0 == cf)) red_finish<128>(r, sel, cf, iw);
        if (p >= 4 && (w == 2u || c0 == cl)) {
          if (!poll_ok(r)) flag_wait(r, cl * 4u + lw);
          red_issue<128>(r, cl, iw);
        }
        if (p <= 5 && (w == 2u || c0 == cp)) poll_issue(r, cp * 4u + lw);
      }
    }
    halfY<NB, 1>(e, fr, b0, (unsigned)(2 * p) * 1024u);
    pv = sl; sl = sl == 4u * SLOT ? 0u : sl + SLOT;
    PRE(45 + p);
    if (p < 4) asm volatile("s_barrier" ::: "memory");
    POST(45 + p); RT(45 + p);
  }
  {
    asm volatile("s_nop 7" ::: "memory");
    FENCE();
    u32x4 t[2];
    sfor<NB * 13>([&](auto K) AI { drain_op<NB, 1, K>(e, t, 13u * 1024u); });
  }
  wait_vm<2 * NB>();
  flag_set(r, 5u * 4u + w);
  wait_vm<1>();
  flag_set(r, 6u * 4u + w);
  POST(52); RT(52);
  if constexpr (NB == 2) {
    const unsigned ca = w + 1u, cb = w + 3u;
    flag_wait(r, ca * 4u + c0); red_issue<192>(r, ca, h);
    if (w == 2u || c0 == 2u) red_finish<128>(r, sel, 2u, iw);
    POST(55); RT(55);
    flag_wait(r, cb * 4u + c0); red_issue<128>(r, cb, h);
    POST(56); RT(56);
    if (w == 2u && h == 0) { u32x2 tg = {tlo, thi}; st8_sc1(tg, laneE == 0u ? 0u : OOB, 28u * 128u, srdF); }
    red_finish<192>(r, sel, ca, h);
    wait_vm<1>();
    red_finish<128>(r, sel, cb, h);
  } else {
    if (w == 0u) {
      const unsigned cB = c0 == 3u ? 3u : c0 + 4u;
      flag_wait(r, cB * 4u + (ib >> 1) - 8u); red_issue<128>(r, cB, ib);
      wait_vm<0>();
      red_finish<128>(r, sel, cB, ib);
    }
  }
}

__global__ __launch_bounds__(256, 1)
void attn_kernel(const __hip_bfloat16* __restrict__ xA, const __hip_bfloat16* __restrict__ kA, const __hip_bfloat16* __restrict__ vA,
                 const __hip_bfloat16* __restrict__ wqA, const __hip_bfloat16* __restrict__ woA,
                 const __hip_bfloat16* __restrict__ xB, const __hip_bfloat16* __restrict__ kB, const __hip_bfloat16* __restrict__ vB,
                 const __hip_bfloat16* __restrict__ wqB, const __hip_bfloat16* __restrict__ woB, char* __restrict__ ws, __hip_bfloat16* __restrict__ out) {
  extern __shared__ __attribute__((aligned(16))) char lds[];
  uint64_t pc0; asm volatile("s_getpc_b64 %0" : "=s"(pc0));
  const unsigned blk = blockIdx.x, xcd = blk & 7u, j = blk >> 3;
  const int br = xcd & 1u, h = (xcd >> 1) * 4 + (j & 3u), b = j >> 2;
  const unsigned lane = threadIdx.x & 63u, w = rfl(threadIdx.x >> 6);
  const unsigned lds0 = (unsigned)(size_t)lds;
  const __hip_bfloat16* x  = br ? xB : xA;
  const __hip_bfloat16* kc = br ? kB : kA;
  const __hip_bfloat16* vc = br ? vB : vA;
  const __hip_bfloat16* wq = br ? wqB : wqA;
  const __hip_bfloat16* wo = br ? woB : woA;
  if (w < 2) body<3>(x, kc, vc, wq, wo, ws, out, br, b, h, j, w, lane, lds0, pc0);
  else       body<2>(x, kc, vc, wq, wo, ws, out, br, b, h, j, w, lane, lds0, pc0);
}

extern "C" void kernel_launch(void* const* d_in, const int* in_sizes, int n_in, void* d_out, int out_size, void* d_ws, size_t ws_size,
                              hipStream_t stream) {
  (void)in_sizes; (void)n_in; (void)out_size; (void)ws_size;
  typedef const __hip_bfloat16* P;
  (void)hipFuncSetAttribute((const void*)attn_kernel, hipFuncAttributeMaxDynamicSharedMemorySize, 163840);
  attn_kernel<<<256, 256, 163840, stream>>>((P)d_in[0], (P)d_in[1], (P)d_in[2], (P)d_in[3], (P)d_in[4],
                                            (P)d_in[5], (P)d_in[6], (P)d_in[7], (P)d_in[8], (P)d_in[9], (char*)d_ws, (__hip_bfloat16*)d_out);
}
```

```cpp
#include <hip/hip_runtime.h>
#include <hip/hip_bf16.h>
#include <utility>
#include <stdint.h>

#define NTX ""
#define NTP ""
#define NTW false
#define NTE true
#define NTR " nt"
#define NTS ""
typedef float    f32x4  __attribute__((ext_vector_type(4)));
typedef float    f32x2  __attribute__((ext_vector_type(2)));
typedef __bf16   bf16x8 __attribute__((ext_vector_type(8)));
typedef __bf16   bf16x4 __attribute__((ext_vector_type(4)));
typedef __bf16   bf16x2 __attribute__((ext_vector_type(2)));
typedef unsigned u32x4  __attribute__((ext_vector_type(4)));
typedef unsigned u32x2  __attribute__((ext_vector_type(2)));

#define DI __device__ __forceinline__
#define LDSP __attribute__((address_space(3)))
#define FENCE() __builtin_amdgcn_sched_barrier(0)
#define AI __attribute__((always_inline))

template <class F, int... Is> DI void sfor_(F&& f, std::integer_sequence<int, Is...>) { (f(std::integral_constant<int, Is>{}), ...); }
template <int N, class F> DI void sfor(F&& f) { sfor_(f, std::make_integer_sequence<int, N>{}); }
template <int V> using IC = std::integral_constant<int, V>;

constexpr int SQ = 160, DM = 448, NH = 16, SK = 1024, DH = 256;
constexpr unsigned SLOT = 32768u;
constexpr float SCL = 0.09016844005556021f;
constexpr float THR = 8.0f;
constexpr unsigned SRD3 = 0x00020000u;
constexpr unsigned PARTB = 140u * 1024u;
constexpr size_t FLAG_OFF = 40u << 20;
constexpr unsigned OOB = 0x40000000u;
constexpr unsigned CODE_PF = 64u * 1024u;

constexpr unsigned QSL(int C) { return (C % 3 == 0 ? 1u : (C % 3 == 1 ? 4u : 0u)) * SLOT; }

DI unsigned lane_id() { unsigned l; asm volatile("v_mbcnt_lo_u32_b32 %0, -1, 0\n\tv_mbcnt_hi_u32_b32 %0, -1, %0" : "=v"(l)); return l; }
DI unsigned rfl(unsigned x) { return __builtin_amdgcn_readfirstlane(x); }
DI u32x4 mk_srd(const void* p, unsigned bytes) {
  const uint64_t a = (uint64_t)p;
  u32x4 r; r[0] = rfl((unsigned)a); r[1] = rfl((unsigned)(a >> 32)); r[2] = bytes; r[3] = SRD3; return r;
}
DI unsigned cvtpk(float lo, float hi) { f32x2 v = {lo, hi}; bf16x2 b = __builtin_convertvector(v, bf16x2); return __builtin_bit_cast(unsigned, b); }
DI float max3f(float a, float b, float c) { float r; asm("v_max3_f32 %0, %1, %2, %3" : "=v"(r) : "v"(a), "v"(b), "v"(c)); return r; }
DI float max2f(float a, float b) { float r; asm("v_max_f32_e32 %0, %1, %2" : "=v"(r) : "v"(a), "v"(b)); return r; }
DI float fma_s(float a, float b, float c) { float r; asm("v_fma_f32 %0, %1, %2, %3" : "=v"(r) : "v"(a), "s"(b), "v"(c)); return r; }
DI void fma_ip(float& x, float s, float c) { asm volatile("v_fma_f32 %0, %0, %1, %2" : "+v"(x) : "s"(s), "v"(c)); }
DI void exp_ip(float& x) { asm volatile("v_exp_f32_e32 %0, %0" : "+v"(x)); }
DI unsigned cvt_v(float lo, float hi) { unsigned r; asm volatile("v_cvt_pk_bf16_f32 %0, %1, %2" : "=v"(r) : "v"(lo), "v"(hi)); return r; }
DI float mul_v(float a, float b) { float r; asm volatile("v_mul_f32_e32 %0, %1, %2" : "=v"(r) : "v"(a), "v"(b)); return r; }

DI void mf_vv(f32x4& d, const bf16x8& a, const bf16x8& b) { asm volatile("v_mfma_f32_16x16x32_bf16 %0, %1, %2, %0" : "+v"(d) : "v"(a), "v"(b)); }
DI void mf_vv0(f32x4& d, const bf16x8& a, const bf16x8& b) { asm volatile("v_mfma_f32_16x16x32_bf16 %0, %1, %2, 0" : "=&v"(d) : "v"(a), "v"(b)); }

template <int MO, bool NT> DI unsigned dma(unsigned mb, unsigned so, unsigned step, unsigned voff, const u32x4& srd) {
  unsigned nso;
  if constexpr (NT) asm volatile("s_add_u32 m0, %1, %2\n\ts_add_u32 %0, %3, %4\n\tbuffer_load_dwordx4 %5, %6, %3 offen nt lds" : "=&s"(nso) : "s"(mb), "i"(MO), "s"(so), "s"(step), "v"(voff), "s"(srd) : "scc");
  else              asm volatile("s_add_u32 m0, %1, %2\n\ts_add_u32 %0, %3, %4\n\tbuffer_load_dwordx4 %5, %6, %3 offen lds"    : "=&s"(nso) : "s"(mb), "i"(MO), "s"(so), "s"(step), "v"(voff), "s"(srd) : "scc");
  return nso;
}
DI bf16x8 lds_b128(unsigned addr) { return *(const LDSP bf16x8*)addr; }
DI bf16x8 lds_tr2(unsigned addr0, unsigned addr1) {
  bf16x4 lo = __builtin_amdgcn_ds_read_tr16_b64_v4bf16((LDSP bf16x4*)addr0);
  bf16x4 hi = __builtin_amdgcn_ds_read_tr16_b64_v4bf16((LDSP bf16x4*)addr1);
  return __builtin_shufflevector(lo, hi, 0, 1, 2, 3, 4, 5, 6, 7);
}
DI bf16x4 lds_tr(unsigned addr) { return __builtin_amdgcn_ds_read_tr16_b64_v4bf16((LDSP bf16x4*)addr); }
DI bf16x8 cat8(const bf16x4& lo, const bf16x4& hi) { return __builtin_shufflevector(lo, hi, 0, 1, 2, 3, 4, 5, 6, 7); }
DI void pf_load(unsigned& dummy, unsigned vo, const u32x4& srd) { asm volatile("buffer_load_dword %0, %1, %2, 0 offen" : "+v"(dummy) : "v"(vo), "s"(srd)); }
DI void ld8_sc1(u32x2& d, unsigned vo, const u32x4& srd) { asm volatile("buffer_load_dwordx2 %0, %1, %2, 0 offen sc1" : "=v"(d) : "v"(vo), "s"(srd)); }
DI void st8_sc1(const u32x2& d, unsigned vo, unsigned so, const u32x4& srd) { asm volatile("buffer_store_dwordx2 %0, %1, %2, %3 offen sc1" :: "v"(d), "v"(vo), "s"(srd), "s"(so) : "memory"); }
DI void st16_sc1(const u32x4& d, unsigned vo, unsigned so, const u32x4& srd) { asm volatile("buffer_store_dwordx4 %0, %1, %2, %3 offen sc1" NTS :: "v"(d), "v"(vo), "s"(srd), "s"(so) : "memory"); }
DI void st16(const u32x4& d, unsigned vo, unsigned so, const u32x4& srd) { asm volatile("buffer_store_dwordx4 %0, %1, %2, %3 offen" :: "v"(d), "v"(vo), "s"(srd), "s"(so) : "memory"); }
#define XCLOB "a0","a1","a2","a3","a4","a5","a6","a7","a8","a9","a10","a11","a12","a13","a14","a15","a16","a17","a18","a19","a20","a21","a22","a23", \
  "a24","a25","a26","a27","a28","a29","a30","a31","a32","a33","a34","a35","a36","a37","a38","a39","a40","a41","a42","a43","a44","a45","a46","a47", \
  "a48","a49","a50","a51","a52","a53","a54","a55","a56","a57","a58","a59","a60","a61","a62","a63","a64","a65","a66","a67","a68","a69","a70","a71", \
  "a72","a73","a74","a75","a76","a77","a78","a79","a80","a81","a82","a83","a84","a85","a86","a87","a88","a89","a90","a91","a92","a93","a94","a95", \
  "a96","a97","a98","a99","a100","a101","a102","a103","a104","a105","a106","a107","a108","a109","a110","a111","a112","a113","a114","a115","a116","a117","a118","a119", \
  "a120","a121","a122","a123","a124","a125","a126","a127","a128","a129","a130","a131","a132","a133","a134","a135","a136","a137","a138","a139","a140","a141","a142","a143", \
  "a144","a145","a146","a147","a148","a149","a150","a151","a152","a153","a154","a155","a156","a157","a158","a159","a160","a161","a162","a163","a164","a165","a166","a167", \
  "a168","a169","a170","a171","a172","a173","a174","a175","a176","a177","a178","a179","a180","a181","a182","a183","a184","a185","a186","a187","a188","a189","a190","a191", \
  "a192","a193","a194","a195","a196","a197","a198","a199","a200","a201","a202","a203","a204","a205","a206","a207","a208","a209","a210","a211","a212","a213","a214","a215", \
  "a216","a217","a218","a219","a220","a221","a222","a223","a224","a225","a226","a227","a228","a229","a230","a231","a232","a233","a234","a235","a236","a237","a238","a239", \
  "a240","a241","a242","a243","a244","a245","a246","a247","a248","a249","a250","a251","a252","a253","a254","a255"
template <int R, int OFF> DI void xa_load(unsigned vo, const u32x4& srd) {
  asm volatile("buffer_load_dwordx4 a[%c0:%c1], %2, %3, 0 offen offset:%4" NTX :: "i"(R), "i"(R + 3), "v"(vo), "s"(srd), "i"(OFF) : XCLOB); }
template <int R> DI void xa_perm(unsigned pa) { asm volatile("ds_bpermute_b32 a%c0, %1, a%c0" :: "i"(R), "v"(pa) : XCLOB); }
template <int R> DI void mf_vx(f32x4& d, const bf16x8& a) { asm volatile("v_mfma_f32_16x16x32_bf16 %0, %1, a[%c2:%c3], %0" : "+v"(d) : "v"(a), "i"(R), "i"(R + 3)); }
template <int R> DI void mf_vx0(f32x4& d, const bf16x8& a) { asm volatile("v_mfma_f32_16x16x32_bf16 %0, %1, a[%c2:%c3], 0" : "=&v"(d) : "v"(a), "i"(R), "i"(R + 3)); }
template <int NB> constexpr int XREG(int ks, int blk) { return NB == 3 ? (ks < 10 ? 12 * ks : (ks < 12 ? 192 + 12 * (ks - 10) : 224 + 12 * (ks - 12))) + 4 * blk : 8 * ks + 4 * blk; }
constexpr int OREG(int blk, int db) { return 64 * blk + 4 * db; }
template <int NB> constexpr int QREG(int sp) { return (NB == 3 ? 192 : 128) + 4 * sp; }
template <int R> DI void mf_o(const bf16x8& a, const bf16x8& b) { asm volatile("v_mfma_f32_16x16x32_bf16 a[%c2:%c3], %0, %1, a[%c2:%c3]" :: "v"(a), "v"(b), "i"(R), "i"(R + 3)); }
template <int R> DI void acc_zero() { asm volatile("v_accvgpr_write_b32 a%c0, 0" :: "i"(R)); }
template <int R> DI void acc_wr(unsigned v) { asm volatile("v_accvgpr_write_b32 a%c1, %0" :: "v"(v), "i"(R)); }
template <int R> DI float acc_rd() { float r; asm volatile("v_accvgpr_read_b32 %0, a%c1" : "=v"(r) : "i"(R)); return r; }
template <int R> DI void acc_mul(float f) { float t; asm volatile("v_accvgpr_read_b32 %0, a%c2\n\tv_mul_f32_e32 %0, %1, %0\n\tv_accvgpr_write_b32 a%c2, %0" : "=&v"(t) : "v"(f), "i"(R)); }
DI void pfa_load(unsigned vo, unsigned so, const u32x4& srd) { asm volatile("buffer_load_dword a255, %0, %1, %2 offen" :: "v"(vo), "s"(srd), "s"(so)); }
template <int OFF> DI void pf4_load(unsigned vo, unsigned so, const u32x4& srd) {
  asm volatile("buffer_load_dwordx4 a[252:255], %0, %1, %2 offen offset:%3" NTP :: "v"(vo), "s"(srd), "s"(so), "i"(OFF)); }
template <int OFF> DI void pf4_keep(unsigned vo, unsigned so, const u32x4& srd) {
  asm volatile("buffer_load_dwordx4 a[252:255], %0, %1, %2 offen offset:%3" :: "v"(vo), "s"(srd), "s"(so), "i"(OFF)); }
template <int IMM> DI void vadd_ip(unsigned& x) { asm volatile("v_add_u32 %0, %1, %0" : "+v"(x) : "i"(IMM)); }
template <int IMM> DI void sadd_ip(unsigned& x) { asm volatile("s_add_u32 %0, %0, %1" : "+s"(x) : "i"(IMM) : "scc"); }
template <int IMM> DI void m0_set(unsigned mb) { asm volatile("s_add_u32 m0, %0, %1" :: "s"(mb), "i"(IMM) : "scc"); }
template <int GO, int OFF> DI void dma_nt(unsigned vo, unsigned sbase, const u32x4& srd) {
  unsigned so; asm volatile("s_add_u32 %0, %2, %3\n\tbuffer_load_dwordx4 %1, %4, %0 offen offset:%5 nt lds" : "=&s"(so) : "v"(vo), "s"(sbase), "i"(GO), "s"(srd), "i"(OFF) : "scc"); }
template <int R, int OFF> DI void tr_acc(unsigned addr) { asm volatile("ds_read_b64_tr_b16 a[%c0:%c1], %2 offset:%3" :: "i"(R), "i"(R + 1), "v"(addr), "i"(OFF)); }
template <int RA, int RB> DI void mf_xx(f32x4& d) { asm volatile("v_mfma_f32_16x16x32_bf16 %0, a[%c1:%c2], a[%c3:%c4], %0" : "+v"(d) : "i"(RA), "i"(RA + 3), "i"(RB), "i"(RB + 3)); }
template <int RA, int RB> DI void mf_xx0(f32x4& d) { asm volatile("v_mfma_f32_16x16x32_bf16 %0, a[%c1:%c2], a[%c3:%c4], 0" : "=&v"(d) : "i"(RA), "i"(RA + 3), "i"(RB), "i"(RB + 3)); }
template <int N> DI void wait_lgkm() { asm volatile("s_waitcnt lgkmcnt(%0)" :: "i"(N)); }
constexpr int QPD = 5, QFB = 32 - QPD;
template <int NB> constexpr bool q_bpa(int C, int f) { return NB == 3 ? (C < 6 && (f & 1) == 1 && f / 2 < 13) : (C < 6 && f < 16); }
template <int NB> constexpr bool q_bpb(int C, int f) { return NB == 3 && C < 6 && (f & 1) == 0 && f / 2 < 11; }
constexpr int q_nvw(int C) { return C <= 4 ? 8 : 0; }
template <int NB> constexpr int q_nvx(int C) { return C <= 3 ? 2 * NB : 0; }
constexpr int q_kv0(int C) { return C == 4 ? 0 : (C == 5 ? 4 : 12); }
constexpr int q_nvk(int C) { return C < 4 ? 0 : (C == 4 ? 4 : 8); }
template <int NB> constexpr int q_vat(int C, int f) {
  const int nv = q_nvw(C) + q_nvx<NB>(C) + q_nvk(C);
  if (f >= QFB || (f & 1)) return -1;
  if (nv > 8) return f / 2 < nv ? f / 2 : -1;
  constexpr int p8[8] = {0, 4, 8, 10, 14, 18, 22, 26};
  for (int v = 0; v < nv; ++v) if (p8[v] == f) return v;
  return -1;
}
template <int NB> constexpr int q_cv0(int f) {
  int k = 0;
  for (int g = 6; g < f; ++g) k += NB == 3 ? (q_vat<NB>(6, g) >= 0 ? 1 : 3) : 4;
  return k;
}
template <int NB> struct QSched {
  int wait[224];
  constexpr QSched() : wait{} {
    int pos[224 + QPD] = {}; int n = 0, bar = 0;
    for (int F = 0; F < QPD; ++F) { n += 2; pos[F] = n; }
    for (int F = 0; F < 224; ++F) {
      const int C = F / 32, f = F % 32;
      if (f == QFB && C < 6) bar = n;
      wait[F] = pos[F] <= bar ? -1 : n - pos[F];
      const bool nxt = F + QPD < 224;
      if (NB == 3) { n += q_bpa<NB>(C, f); if (nxt) n += 2; pos[F + QPD] = n; n += q_bpb<NB>(C, f); }
      else { if (nxt) n += 1; n += q_bpa<NB>(C, f); if (nxt) n += 1; pos[F + QPD] = n; }
    }
  }
};
DI unsigned mov_v(unsigned a) { unsigned r; asm volatile("v_mov_b32 %0, %1" : "=v"(r) : "v"(a)); return r; }
DI void swap32(unsigned& a, unsigned& b) { asm volatile("v_permlane32_swap_b32 %0, %1" : "+v"(a), "+v"(b)); }
DI void swap16(unsigned& a, unsigned& b) { asm volatile("v_permlane16_swap_b32 %0, %1" : "+v"(a), "+v"(b)); }
DI void tie_v8(bf16x8& d) { asm volatile("" : "+v"(d)); }
DI void tie_v4(f32x4& d) { asm volatile("" : "+v"(d)); }
DI void tie_v2(u32x2& d) { asm volatile("" : "+v"(d)); }
DI void tie_v(unsigned& d) { asm volatile("" : "+v"(d)); }
template <int N> DI void wait_vm() { asm volatile("s_waitcnt vmcnt(%0)" :: "i"(N) : "memory"); }
template <int N> DI void wait_bar() { asm volatile("s_waitcnt vmcnt(%0) lgkmcnt(0)\n\ts_barrier" :: "i"(N) : "memory", XCLOB); }

#define RTL(i)
struct Prof {};
#define PRE(i)
#define POST(i)
#define RT(i)

struct Desc { u32x4 srd; unsigned so, inc, ginc, mb; unsigned v[4]; };

template <int NB> struct Ctx {
  f32x4  L[NB];
  bf16x8 Q[NB][8];
  f32x4  S[NB][4];
  u32x4  P[NB][2];
  float  nm[NB], thr[NB], dl[NB], mx[NB];
  bf16x8 ones;
  unsigned kbase, vbase;
};

template <int I> DI void dma_m0(const Desc& d) { asm volatile("s_add_u32 m0, %0, %1" :: "s"(d.mb), "i"(16384 * (I >> 2)) : "scc"); }
template <int I, bool NT> DI void dma_ld(const Desc& d) {
  if constexpr (NT) asm volatile("buffer_load_dwordx4 %0, %1, %2 offen offset:%3 nt lds" :: "v"(d.v[I & 3]), "s"(d.srd), "s"(d.so), "i"(1024 * (I & 3)));
  else              asm volatile("buffer_load_dwordx4 %0, %1, %2 offen offset:%3 lds"    :: "v"(d.v[I & 3]), "s"(d.srd), "s"(d.so), "i"(1024 * (I & 3)));
}
template <int I> DI void dma_adv(Desc& d) { if constexpr (I < 7) asm volatile("s_add_u32 %0, %0, %1" : "+s"(d.so) : "s"((I & 3) == 3 ? d.ginc : d.inc) : "scc"); }
template <int I, bool NT> DI void dma_piece(Desc& d) {
  if constexpr ((I & 3) == 0) { dma_m0<I>(d); asm volatile("s_nop 0"); }
  dma_ld<I, NT>(d); dma_adv<I>(d);
}

template <int NB, int KB0, int K> DI void smF(Ctx<NB>& c) { constexpr int kb = KB0 + K / (4 * NB), blk = (K % (4 * NB)) / 4, r = K % 4;
  float x = c.S[blk][kb][r]; fma_ip(x, SCL, c.nm[blk]); c.S[blk][kb][r] = x; }
template <int NB, int KB0, int K> DI void smE(Ctx<NB>& c) { constexpr int kb = KB0 + K / (4 * NB), blk = (K % (4 * NB)) / 4, r = K % 4;
  float x = c.S[blk][kb][r]; exp_ip(x); c.S[blk][kb][r] = x; }
template <int NB, int KB0, int J> DI void smC(Ctx<NB>& c) { constexpr int ko = J / (2 * NB), blk = (J % (2 * NB)) / 2, pr = J % 2;
  c.P[blk][KB0 >> 1][2 * ko + pr] = cvt_v(c.S[blk][KB0 + ko][2 * pr], c.S[blk][KB0 + ko][2 * pr + 1]); }
template <int NB, int KB0, int K> DI void sm_gen(Ctx<NB>& c) {
  constexpr int j = K / 5, pos = K % 5;
  if constexpr (pos < 2) { if constexpr (j < 2 * NB * 2) smF<NB, KB0, 2 * j + pos>(c); }
  else if constexpr (pos < 4) { if constexpr (j < 2 * NB * 2) smE<NB, KB0, 2 * j + pos - 2>(c); }
  else if constexpr (j >= 1 && j - 1 < 2 * NB * 2) smC<NB, KB0, j - 1>(c);
}

DI float max3v(float a, float b, float c) { float r; asm volatile("v_max3_f32 %0, %1, %2, %3" : "=v"(r) : "v"(a), "v"(b), "v"(c)); return r; }
template <int NB, int J> DI void smM(Ctx<NB>& c) { constexpr int kb = J / (2 * NB), hf = (J % (2 * NB)) / NB, blk = J % NB;
  if constexpr (kb == 0 && hf == 0) c.mx[blk] = max3v(c.S[blk][0][0], c.S[blk][0][1], c.S[blk][0][1]);
  else c.mx[blk] = max3v(c.mx[blk], c.S[blk][kb][2 * hf], c.S[blk][kb][2 * hf + 1]); }

template <int NB, bool FIRST, bool NT> DI void phaseA(Ctx<NB>& c, unsigned slot, Desc& d, Prof& pf, int si) {
  PRE(si); wait_bar<16>(); POST(si); RTL(si);
  const unsigned b0 = c.kbase + slot;
  unsigned kb4[4] = {b0, b0 ^ 64u, b0 ^ 128u, b0 ^ 192u};
#define PDA3 4
#define PDA2 3
  constexpr int PD = NB == 3 ? PDA3 : PDA2, RG = PD + 1;
  bf16x8 fr[RG];
  auto rd = [&](auto F) AI { constexpr int f = F, kb = f >> 3, s = f & 7; fr[f % RG] = lds_b128(kb4[s & 3] + 256 * (s >> 2) + 8192 * kb); };
  sfor<PD>(rd);
  dma_m0<0>(d);
  FENCE();
  auto oz = [&](auto Z) AI { constexpr int z = Z;
    if constexpr (z < NB * 64) acc_zero<z>(); };
  auto mf = [&](auto F, auto Bk) AI {
    constexpr int f = F, kb = f >> 3, s = f & 7, blk = Bk;
    if constexpr (blk == 0) { if constexpr (s == 0) mf_vx0<QREG<NB>(s)>(c.S[0][kb], fr[f % RG]); else mf_vx<QREG<NB>(s)>(c.S[0][kb], fr[f % RG]); }
    else                    { if constexpr (s == 0) mf_vv0(c.S[blk][kb], fr[f % RG], c.Q[blk][s]); else mf_vv(c.S[blk][kb], fr[f % RG], c.Q[blk][s]); }
    FENCE();
  };
  auto dsal = [&](auto F) AI { constexpr int f = F;
    if constexpr ((f & 3) == 2) dma_adv<(f >> 2)>(d);
    if constexpr (f == 16) dma_m0<4>(d); };
  if constexpr (NB == 3) {
    if constexpr (!FIRST) {
      sfor<6>([&](auto K) AI { smF<3, 2, K>(c); });
      sfor<4>([&](auto K) AI { smE<3, 2, K>(c); });
      smC<3, 2, 0>(c);
      FENCE();
    }
    sfor<32>([&](auto F) AI {
      constexpr int f = F;
      mf(F, IC<0>{});
      if constexpr (FIRST) { oz(IC<6 * f>{}); oz(IC<6 * f + 1>{}); }
      else if constexpr (f < 20) smE<3, 2, f + 4>(c);
      else if constexpr (f < 23) { smM<3, 6 + 2 * (f - 20)>(c); smM<3, 7 + 2 * (f - 20)>(c); }
      else if constexpr (f >= 26 && f < 29) { smM<3, 12 + 2 * (f - 26)>(c); smM<3, 13 + 2 * (f - 26)>(c); }
      FENCE();
      mf(F, IC<1>{});
      if constexpr (f + PD < 32) rd(IC<f + PD>{});
      FENCE();
      if constexpr (FIRST) { oz(IC<6 * f + 2>{}); oz(IC<6 * f + 3>{}); }
      else if constexpr (f + 6 < 24) smF<3, 2, f + 6>(c);
      FENCE();
      mf(F, IC<2>{});
      if constexpr ((f & 3) == 1) dma_ld<(f >> 2), NT>(d);
      if constexpr (FIRST) { oz(IC<6 * f + 4>{}); oz(IC<6 * f + 5>{}); }
      else if constexpr (!(f & 1) && f / 2 + 1 < 12) smC<3, 2, f / 2 + 1>(c);
      else if constexpr (f == 11 || f == 15 || f == 19) { smM<3, 2 * ((f - 11) / 4)>(c); smM<3, 2 * ((f - 11) / 4) + 1>(c); }
      dsal(F);
      FENCE();
    });
  } else {
    constexpr int NOPS = NB * 4 * 5 + 5, GF = 22 * NB;
    sfor<32>([&](auto F) AI {
      constexpr int f = F;
      sfor<NB>([&](auto Bk) AI {
        constexpr int blk = Bk, g = f * NB + blk;
        mf(F, Bk);
        if constexpr (blk == 0 && f + PD < 32) rd(IC<f + PD>{});
        if constexpr (blk == 1 && (f & 3) == 1) dma_ld<(f >> 2), NT>(d);
        FENCE();
        if constexpr (FIRST) { oz(IC<2 * g>{}); oz(IC<2 * g + 1>{}); }
        else {
          constexpr int lo = (g < GF) ? (g * NOPS) / GF : NOPS, hi = (g + 1 < GF) ? ((g + 1) * NOPS) / GF : NOPS;
          sfor<hi - lo>([&](auto J) AI { sm_gen<NB, 2, lo + J>(c); });
          if constexpr (g >= GF && g < GF + 4 * NB) smM<NB, g - GF>(c);
          if constexpr (g >= 26 * NB && g < 28 * NB) smM<NB, 4 * NB + g - 26 * NB>(c);
        }
        if constexpr (blk == 1) dsal(F);
        FENCE();
      });
    });
  }
}

template <int NB> DI float rowmax16(const f32x4* s) {
  float m = max3f(s[0][0], s[0][1], s[0][2]);
  m = max3f(m, s[0][3], s[1][0]); m = max3f(m, s[1][1], s[1][2]); m = max3f(m, s[1][3], s[2][0]);
  m = max3f(m, s[2][1], s[2][2]); m = max3f(m, s[2][3], s[3][0]); m = max3f(m, s[3][1], s[3][2]);
  return max2f(m, s[3][3]);
}
DI float xl32(float v) { auto p = __builtin_amdgcn_permlane32_swap(__float_as_uint(v), __float_as_uint(v), false, false); return max2f(__uint_as_float(p[0]), __uint_as_float(p[1])); }
DI float xl16(float v) { auto p = __builtin_amdgcn_permlane16_swap(__float_as_uint(v), __float_as_uint(v), false, false); return max2f(__uint_as_float(p[0]), __uint_as_float(p[1])); }
constexpr float THR_RAW = THR / SCL;

template <int NB> DI void phaseB0(Ctx<NB>& c) {
  asm volatile("s_nop 7" ::: "memory");
  FENCE();
  sfor<NB>([&](auto Bk) AI { constexpr int blk = Bk; c.mx[blk] = rowmax16<NB>(c.S[blk]); });
  sfor<NB>([&](auto Bk) AI { constexpr int blk = Bk; const float m = xl16(xl32(c.mx[blk])); c.nm[blk] = -SCL * m; c.thr[blk] = m + THR_RAW; c.dl[blk] = 0.f;
    c.L[blk] = f32x4{0.f, 0.f, 0.f, 0.f}; });
  sfor<NB * 20 + 5>([&](auto J) AI { sm_gen<NB, 0, J>(c); });
}

#define BDMA 1
template <int NB, bool FINAL, bool NT> DI void phaseB(Ctx<NB>& c, unsigned slot, Desc& d, Prof& pf, int si) {
  PRE(si); wait_bar<16>(); POST(si); RTL(si);
  const unsigned b0 = c.vbase + slot;
#define PDB3 4
#define PDB2 3
  constexpr int PD = NB == 3 ? PDB3 : PDB2, RG = PD + 1;
  bf16x4 flo[RG], fhi[RG];
  unsigned vb[8];
  auto rdl = [&](auto F) AI { constexpr int f = F, u = f >> 4, j = (f >> 1) & 7, hi = f & 1; flo[f % RG] = lds_tr(vb[j] + 256 * hi + 16384 * u); };
  auto rdh = [&](auto F) AI { constexpr int f = F, u = f >> 4, j = (f >> 1) & 7, hi = f & 1; fhi[f % RG] = lds_tr(vb[j] + 256 * hi + 16384 * u + 8192); };
  vb[0] = b0; vb[1] = b0 ^ 32u; vb[2] = b0 ^ 64u; vb[3] = b0 ^ 96u;
  sfor<PD>([&](auto F) AI { rdl(F); rdh(F); });
  asm volatile("" ::: "memory");
  FENCE();
  dma_m0<0>(d);
  sfor<6>([&](auto J) AI { if constexpr (2 * (2 + J) >= PD) { vb[2 + J] = b0 ^ (32u * (2 + J)); tie_v(vb[2 + J]); } });
  unsigned long long chg = 0;
  if constexpr (!FINAL) {
    sfor<2 * NB>([&](auto J) AI { smM<NB, 6 * NB + J>(c); });
    sfor<NB>([&](auto Bk) AI { constexpr int blk = Bk; chg |= __ballot(c.mx[blk] > c.thr[blk]); });
    if (__builtin_expect(chg != 0ull, 0)) {
      sfor<NB>([&](auto Bk) AI { constexpr int blk = Bk;
        const float m = xl16(xl32(c.mx[blk]));
        const float t = fma_s(m, SCL, c.nm[blk]);
        const bool mv = m > c.thr[blk];
        c.dl[blk] = mv ? t : 0.f;
        c.nm[blk] -= c.dl[blk];
        c.thr[blk] = mv ? m + THR_RAW : c.thr[blk]; });
    }
  }
  FENCE();
  auto mfl = [&](auto U, auto Bk) AI { mf_vv(c.L[Bk], c.ones, __builtin_bit_cast(bf16x8, c.P[Bk][U])); FENCE(); };
  auto mfo = [&](auto F, auto Bk) AI { constexpr int f = F, u = f >> 4, db = ((f >> 1) & 7) + 8 * (f & 1);
    mf_o<OREG(Bk, db)>(cat8(flo[f % RG], fhi[f % RG]), __builtin_bit_cast(bf16x8, c.P[Bk][u])); FENCE(); };
  auto dsal = [&](auto F) AI { constexpr int f = F;
    if constexpr ((f & 3) == 2) dma_adv<(f >> 2)>(d);
    if constexpr (f == 16) dma_m0<4>(d); };
  if constexpr (NB == 3 && BDMA == 0) {
    if constexpr (FINAL) {
      sfor<20>([&](auto K) AI { smF<3, 2, K>(c); });
      sfor<9>([&](auto K) AI { smE<3, 2, K>(c); });
      FENCE();
    }
    sfor<2>([&](auto U_) AI {
      constexpr int u = U_;
      sfor<3>([&](auto Bk) AI {
        constexpr int blk = Bk;
        mfl(U_, Bk);
        if constexpr (!FINAL) {
          if constexpr (u == 0) { smF<3, 0, 2 * blk>(c); smF<3, 0, 2 * blk + 1>(c); } else smE<3, 0, 12 + blk>(c);
        } else if constexpr (u == 0) smE<3, 2, 9 + blk>(c);
        FENCE();
      });
      sfor<16>([&](auto FF) AI {
        constexpr int f = 16 * u + FF, nd = f - ((f + 2) >> 2);
        mfo(IC<f>{}, IC<0>{});
        if constexpr ((f & 3) == 1) dma_ld<(f >> 2), NT>(d);
        else if constexpr (!FINAL) { constexpr int e = u == 0 ? nd : nd + 3; if constexpr (e < 24) smE<3, 0, e>(c); }
        else if constexpr (u == 0) smE<3, 2, 12 + nd>(c);
        FENCE();
        mfo(IC<f>{}, IC<1>{});
        if constexpr (f + PD < 32) rdl(IC<f + PD>{});
        FENCE();
        if constexpr (!FINAL) {
          if constexpr (f < 16) smF<3, 0, f + 6>(c);
          else if constexpr (f < 22) smC<3, 0, f - 16>(c);
          else if constexpr (f < 24) smF<3, 0, f>(c);
          else if constexpr (f < 30) smC<3, 0, f - 18>(c);
        } else {
          if constexpr (f < 4) smF<3, 2, 20 + f>(c);
          else if constexpr (f < 16) smC<3, 2, f - 4>(c);
        }
        FENCE();
        mfo(IC<f>{}, IC<2>{});
        if constexpr (f + PD < 32) rdh(IC<f + PD>{});
        dsal(IC<f>{});
        FENCE();
      });
    });
  } else if constexpr (NB == 3) {
    if constexpr (FINAL) {
      sfor<20>([&](auto K) AI { smF<3, 2, K>(c); });
      sfor<9>([&](auto K) AI { smE<3, 2, K>(c); });
      FENCE();
    }
    sfor<2>([&](auto U_) AI {
      constexpr int u = U_;
      sfor<3>([&](auto Bk) AI {
        constexpr int blk = Bk;
        mfl(U_, Bk);
        if constexpr (!FINAL) {
          if constexpr (u == 0) { smF<3, 0, 2 * blk>(c); smF<3, 0, 2 * blk + 1>(c); }
          else if constexpr (blk < 2) smE<3, 0, 16 + blk>(c);
          else { smF<3, 0, 18>(c); smF<3, 0, 19>(c); }
        } else if constexpr (u == 0) smE<3, 2, 9 + blk>(c);
        FENCE();
      });
      sfor<16>([&](auto FF) AI {
        constexpr int f = 16 * u + FF, nd = f - ((f + 2) >> 2);
        constexpr bool dm = (f & 3) == 1;
        mfo(IC<f>{}, IC<0>{});
        if constexpr (!FINAL) {
          if constexpr (f < 16) smE<3, 0, f>(c);
          else if constexpr (f == 16) { smF<3, 0, 20>(c); smF<3, 0, 21>(c); }
          else if constexpr (f == 17) { smF<3, 0, 22>(c); smF<3, 0, 23>(c); }
          else if constexpr (f < 24) smE<3, 0, f>(c);
          else if constexpr (f == 24) { smC<3, 0, 6>(c); smC<3, 0, 7>(c); }
          else if constexpr (f == 25) { smC<3, 0, 9>(c); smC<3, 0, 10>(c); }
          else if constexpr (f == 26) smC<3, 0, 11>(c);
        } else {
          if constexpr (f < 12) smE<3, 2, 12 + f>(c);
          else if constexpr (f == 12) { smC<3, 2, 5>(c); smC<3, 2, 6>(c); }
          else if constexpr (f == 13) { smC<3, 2, 8>(c); smC<3, 2, 9>(c); }
          else if constexpr (f == 14) { smC<3, 2, 10>(c); smC<3, 2, 11>(c); }
        }
        FENCE();
        mfo(IC<f>{}, IC<1>{});
        if constexpr (f + PD < 32) { rdl(IC<f + PD>{}); if constexpr (dm) rdh(IC<f + PD>{}); }
        FENCE();
        if constexpr (!dm) {
          if constexpr (!FINAL) {
            if constexpr (f < 16) smF<3, 0, 6 + nd>(c);
            else if constexpr (f == 16) smC<3, 0, 0>(c);
            else if constexpr (f >= 18 && f <= 20) smC<3, 0, f - 17>(c);
            else if constexpr (f == 22 || f == 23) smC<3, 0, f - 18>(c);
            else if constexpr (f == 24) smC<3, 0, 8>(c);
          } else {
            if constexpr (f < 5) smF<3, 2, 20 + nd>(c);
            else if constexpr (f >= 6 && f <= 8) smC<3, 2, f - 6>(c);
            else if constexpr (f == 10 || f == 11) smC<3, 2, f - 7>(c);
            else if constexpr (f == 12) smC<3, 2, 7>(c);
          }
        }
        FENCE();
        mfo(IC<f>{}, IC<2>{});
        if constexpr (dm) dma_ld<(f >> 2), NT>(d);
        else if constexpr (f + PD < 32) rdh(IC<f + PD>{});
        dsal(IC<f>{});
        FENCE();
      });
    });
  } else {
    constexpr int NU = NB * 4, GH = 17 * NB;
    sfor<2>([&](auto U_) AI {
      constexpr int u = U_;
      sfor<17>([&](auto FF) AI {
        constexpr int ff = FF, f = 16 * u + ff - 1;
        sfor<NB>([&](auto Bk) AI {
          constexpr int blk = Bk, g = u * GH + ff * NB + blk;
          if constexpr (ff == 0) mfl(U_, Bk);
          else {
            mfo(IC<f>{}, Bk);
            if constexpr (f + PD < 32) { if constexpr (blk == 0) rdl(IC<f + PD>{}); else rdh(IC<f + PD>{}); }
            if constexpr (blk == 1 && (f & 3) == 1) dma_ld<(f >> 2), NT>(d);
            if constexpr (blk == 0) dsal(IC<f>{});
            FENCE();
          }
          if constexpr (FINAL) {
            constexpr int NOPS = NU * 5 + 5, GF = GH - 2;
            constexpr int lo = (g < GF) ? (g * NOPS) / GF : NOPS, hi = (g + 1 < GF) ? ((g + 1) * NOPS) / GF : NOPS;
            sfor<hi - lo>([&](auto J) AI { sm_gen<NB, 2, lo + J>(c); });
          } else {
            constexpr int G1 = 2 * GH - 4, NOPS = NU * 4;
            constexpr int lo = g < G1 ? (g * NOPS) / G1 : NOPS, hi = g + 1 < G1 ? ((g + 1) * NOPS) / G1 : NOPS;
            sfor<hi - lo>([&](auto J) AI { constexpr int k = lo + J; sm_gen<NB, 0, (k / 4) * 5 + (k % 4)>(c); });
            sfor<NU>([&](auto UU) AI {
              constexpr int un = UU;
              constexpr int gdone = ((4 * un + 4) * G1 + NOPS - 1) / NOPS + 1, gmin = GH + NB + un;
              constexpr int gat = gdone > gmin ? gdone : gmin;
              if constexpr (g == (gat < 2 * GH - 1 ? gat : 2 * GH - 1)) smC<NB, 0, un>(c);
            });
          }
          FENCE();
        });
      });
    });
  }
  if constexpr (!FINAL) {
    if (__builtin_expect(chg != 0ull, 0)) {
      asm volatile("s_nop 7" ::: "memory");
      FENCE();
      sfor<NB>([&](auto Bk) AI { constexpr int blk = Bk;
        const float f = __builtin_amdgcn_exp2f(-c.dl[blk]);
        c.L[blk] *= f;
        sfor<64>([&](auto Z) AI { acc_mul<OREG(blk, 0) + Z>(f); }); });
      FENCE();
      asm volatile("s_nop 3" ::: "memory");
    }
  }
}

template <int NB> struct Epi {
  bf16x8 of[NB][8];
  f32x4  y[2][NB][2];
  float  rl[NB];
  unsigned vP[NB];
  u32x4  srdP;
};
template <int NB, int BUF, int K> DI void drain_op(Epi<NB>& e, u32x4 (&t)[2], unsigned so) {
  constexpr int blk = K / 13, pos = K % 13;
  if constexpr (pos < 8) { float v = e.y[BUF][blk][pos >> 2][pos & 3]; e.y[BUF][blk][pos >> 2][pos & 3] = mul_v(v, e.rl[blk]); }
  else if constexpr (pos < 12) { constexpr int q = pos - 8; t[blk & 1][q] = cvt_v(e.y[BUF][blk][q >> 1][2 * (q & 1)], e.y[BUF][blk][q >> 1][2 * (q & 1) + 1]); }
  else st16_sc1(t[blk & 1], e.vP[blk], so, e.srdP);
}
template <int NB, int F> DI void yrd(bf16x8 (&fr)[4], unsigned b0) {
  constexpr int nb = F >> 3, s = F & 7; const unsigned a = (b0 ^ (32u * nb)) + 4096 * s; fr[F & 3] = lds_tr2(a, a + 2048);
}
template <int NB, int HF> DI void halfY(Epi<NB>& e, bf16x8 (&fr)[4], unsigned b0, unsigned so) {
  u32x4 t[2];
  constexpr int NOPS = NB * 13, G0 = 2, G1 = 16 * NB - 1;
  FENCE();
  sfor<16>([&](auto F) AI {
    constexpr int fl = F, f = 16 * HF + fl, nb = fl >> 3, s = f & 7;
    sfor<NB>([&](auto Bk) AI {
      constexpr int blk = Bk, g = fl * NB + blk;
      if constexpr (s == 0) mf_vv0(e.y[HF][blk][nb], fr[f & 3], e.of[blk][s]); else mf_vv(e.y[HF][blk][nb], fr[f & 3], e.of[blk][s]);
      if constexpr (blk == 0 && f + 3 < 32) yrd<NB, f + 3>(fr, b0);
      constexpr int lo = g < G0 ? 0 : (g >= G1 ? NOPS : ((g - G0) * NOPS) / (G1 - G0)), hi = g + 1 < G0 ? 0 : (g + 1 >= G1 ? NOPS : ((g + 1 - G0) * NOPS) / (G1 - G0));
      sfor<hi - lo>([&](auto J) AI { drain_op<NB, HF ^ 1, lo + J>(e, t, so); });
      FENCE();
    });
  });
}

struct Red {
  u32x4 srdF, srdR, srdO;
  unsigned tlo, thi;
  unsigned vF;
  unsigned vR, vO;
  unsigned lane;
};
DI void flag_set(const Red& r, unsigned line) {
  u32x2 tg = {r.tlo, r.thi};
  st8_sc1(tg, r.vF, line * 128u, r.srdF);
}
DI void poll_issue(const Red& r, unsigned line) {
  const unsigned vo = r.lane < 16u ? line * 128u + r.lane * 8u : OOB;
  asm volatile("buffer_load_dwordx2 a[126:127], %0, %1, 0 offen sc1" :: "v"(vo), "s"(r.srdF) : "memory", XCLOB);
}
DI bool poll_ok(const Red& r) {
  unsigned lo, hi;
  asm volatile("v_accvgpr_read_b32 %0, a126\n\tv_accvgpr_read_b32 %1, a127" : "=v"(lo), "=v"(hi) :: XCLOB);
  return __ballot(r.lane >= 16u || (lo == r.tlo && hi == r.thi)) == ~0ull;
}
DI void flag_wait(const Red& r, unsigned line) {
#pragma unroll 1
  for (int it = 0; it < (1 << 22); ++it) {
    poll_issue(r, line);
    wait_vm<0>();
    if (poll_ok(r)) break;
    __builtin_amdgcn_s_sleep(1);
  }
}
template <int R, int OFF> DI void pa_load(unsigned vo, unsigned so, const u32x4& srd) {
  asm volatile("buffer_load_dwordx4 a[%c0:%c1], %2, %3, %4 offen offset:%5 sc1" NTR :: "i"(R), "i"(R + 3), "v"(vo), "s"(srd), "s"(so), "i"(OFF) : XCLOB); }
template <int R> DI void red_issue(const Red& r, unsigned c, unsigned idx) {
  const unsigned fr = (idx >> 1) * 14u + 2u * c + (idx & 1u);
  const unsigned so = rfl(fr * 1024u);
  sfor<8>([&](auto J) AI {
    const unsigned s2 = so + 2u * J * PARTB;
    pa_load<R + 8 * J, 0>(r.vR, s2, r.srdR);
    pa_load<R + 8 * J + 4, 256>(r.vR, s2, r.srdR);
  });
}
template <int R> DI void red_finish(const Red& r, const bf16x8& sel, unsigned c, unsigned idx) {
  f32x4 a0, a1;
  asm volatile("" ::: XCLOB);
  sfor<8>([&](auto J) AI {
    if constexpr (J == 0) { mf_vx0<R>(a0, sel); mf_vx0<R + 4>(a1, sel); }
    else { mf_vx<R + 8 * J>(a0, sel); mf_vx<R + 8 * J + 4>(a1, sel); }
  });
  asm volatile("s_nop 7\n\ts_nop 1" ::: "memory", XCLOB);
  FENCE();
  u32x4 t = {cvt_v(a0[0], a0[1]), cvt_v(a0[2], a0[3]), cvt_v(a1[0], a1[1]), cvt_v(a1[2], a1[3])};
  const unsigned so = rfl((idx >> 1) * (16u * 2u * DM * 2u) + (2u * c + (idx & 1u)) * 64u);
  st16(t, r.vO, so, r.srdO);
}

template <int NB>
DI void body(const __hip_bfloat16* __restrict__ x, const __hip_bfloat16* __restrict__ kc, const __hip_bfloat16* __restrict__ vc,
             const __hip_bfloat16* __restrict__ wq, const __hip_bfloat16* __restrict__ wo, char* __restrict__ ws, __hip_bfloat16* __restrict__ out,
             int br, int b, int h, unsigned jx, unsigned w, unsigned lane, unsigned lds0, uint64_t pc0) {
  const int grp = br * 8 + b;
  const unsigned g = lane >> 4, cc = lane & 15u, q4 = cc >> 2, p4 = lane & 3u, hi5 = lane >> 5, ch = lane & 31u;
  Ctx<NB> c;
  const u32x4 srdK = mk_srd((const char*)kc + ((size_t)(b * NH + h) * SK) * (DH * 2), SK * DH * 2);
  const u32x4 srdV = mk_srd((const char*)vc + ((size_t)(b * NH + h) * SK) * (DH * 2), SK * DH * 2);
  const u32x4 srdWq = mk_srd((const char*)wq + h * (DH * 2), DM * NH * DH * 2 - h * (DH * 2));
  const u32x4 srdWo = mk_srd((const char*)wo + (size_t)h * DH * DM * 2, DH * DM * 2);
  const u32x4 srdX = mk_srd((const char*)x + (size_t)b * SQ * DM * 2, SQ * DM * 2);
  const u32x4 srdF = mk_srd(ws + FLAG_OFF + (size_t)grp * 4096, 4096);
  const unsigned rb[3] = {2u * w, 2u * w + 1u, 8u + w};
  Prof pf;
  POST(0); RT(0);

  u32x2 ep;
  ld8_sc1(ep, 28u * 128u, srdF);
  const unsigned mb4 = rfl(lds0 + 4096u * w);
  Desc dQ;
  dQ.srd = srdWq; dQ.inc = 15360u; dQ.ginc = 216064u;
  dQ.v[0] = dQ.v[2] = 8192u * hi5 + 16u * (ch ^ (2u * (hi5 + 4u * (w & 1u)))); dQ.v[1] = dQ.v[3] = dQ.v[0] ^ 64u;
  const unsigned soQ = rfl(65536u * w);
  dQ.mb = mb4 + QSL(0); dQ.so = soQ;
  sfor<8>([&](auto I) AI { dma_piece<I, NTW>(dQ); });
  unsigned vx[NB];
  sfor<NB>([&](auto Bk) AI { vx[Bk] = (16u * rb[Bk] + (lane >> 2)) * (DM * 2) + 16u * (p4 ^ (2u * hi5)); });
  auto xld = [&](auto K_) AI { constexpr int k = K_, ks = k / NB, bk = k % NB; xa_load<XREG<NB>(ks, bk), 64 * ks>(vx[bk], srdX); };
  POST(53);
  sfor<4 * NB>(xld);
  dQ.mb = mb4 + QSL(1); dQ.so = soQ + 64u * 8192u;
  sfor<8>([&](auto I) AI { dma_piece<I, NTW>(dQ); });
  POST(54);
  sfor<2 * NB>([&](auto K) AI { xld(IC<4 * NB + K>{}); });
  const unsigned vlin = lane * 16u;
  if constexpr (NB == 2) {
    if (w == 3) {
      const u32x4 srdC = mk_srd((const void*)(pc0 & ~127ull), CODE_PF);
      const unsigned so = rfl(jx * (CODE_PF / 32u));
      pf4_keep<0>(vlin, so, srdC); pf4_keep<1024>(vlin, so, srdC);
    }
  }
  PRE(1);
  c.kbase = lds0 + 512u * cc + 16u * (g ^ (cc & 3u)) + 64u * (cc >> 2);
  c.vbase = lds0 + 512u * (4u * g + q4) + 32u * (4u * (g & 1u) + q4) + 8u * p4;
  { u32x4 o = {0x3F803F80u, 0x3F803F80u, 0x3F803F80u, 0x3F803F80u}; c.ones = __builtin_bit_cast(bf16x8, o); tie_v8(c.ones); }
  Desc dA, dB;
  dA.srd = srdV; dB.srd = srdK; dA.inc = dB.inc = 0u; dA.ginc = dB.ginc = 16384u;
  sfor<4>([&](auto K) AI { dB.v[K] = 512u * hi5 + 16u * (ch ^ (8u * (w & 1u) + 2u * K + hi5)); dA.v[K] = 512u * hi5 + 16u * (ch ^ (2u * (2u * K + hi5))); });

  unsigned tlo, thi;
  {
    f32x4 qa[NB][16];
    u32x4 qt[NB][8];
    const unsigned wbase = lds0 + 512u * (8u * g + q4) + 32u * (q4 + 4u * (g & 1u)) + 8u * p4;
    const unsigned pa = 4u * (4u * cc + (g ^ (2u * (cc >> 3))));
    auto qcv = [&](auto BB, auto SP, auto K_) AI {
      constexpr int bb = BB, sp = SP, k = K_;
      if constexpr (k < 4) qt[bb][sp][k] = cvt_v(qa[bb][2 * sp + (k >> 1)][2 * (k & 1)], qa[bb][2 * sp + (k >> 1)][2 * (k & 1) + 1]);
      else { unsigned x0 = qt[bb][sp][k & 1], x1 = qt[bb][sp][2 + (k & 1)];
        if constexpr (k < 6) swap32(x0, x1); else swap16(x0, x1);
        qt[bb][sp][k & 1] = x0; qt[bb][sp][2 + (k & 1)] = x1; }
    };
    constexpr int PD = QPD, RG = PD + 1, FB = QFB, RING = NB == 3 ? 120 : 160;
    constexpr QSched<NB> qs{};
    unsigned wb[8];
    sfor<8>([&](auto J) AI { wb[J] = (wbase ^ (32u * J)) + QSL(0); tie_v(wb[J]); });
    auto rdl = [&](auto F) AI { constexpr int f = F & 31, be = f >> 1, u = f & 1; tr_acc<RING + 4 * (F % RG), 256 * (be >> 3) + 16384 * u>(wb[be & 7]); };
    auto rdh = [&](auto F) AI { constexpr int f = F & 31, be = f >> 1, u = f & 1; tr_acc<RING + 4 * (F % RG) + 2, 256 * (be >> 3) + 16384 * u + 2048>(wb[be & 7]); };
    auto xbp = [&](auto C_, auto K) AI { constexpr int C = C_, k = K; xa_perm<XREG<NB>(2 * C + k / (4 * NB), 0) + k % (4 * NB)>(pa); };
    auto kv_pre = [&](auto J) AI { constexpr int j = J, cn = j >> 3, I = j & 7;
      if constexpr (I == 0) { if constexpr (cn < 2) { dB.mb = mb4 + (2 + cn) * SLOT; dB.so = cn * SLOT + 4096u * w; } else { dA.mb = mb4 + 4u * SLOT; dA.so = 4096u * w; } }
      if constexpr ((I & 3) == 0) { if constexpr (cn < 2) dma_m0<I>(dB); else dma_m0<I>(dA); } };
    auto kv_op = [&](auto J) AI { constexpr int j = J, cn = j >> 3, I = j & 7; if constexpr (cn < 2) dma_ld<I, true>(dB); else dma_ld<I, true>(dA); };
    auto kv_post = [&](auto J) AI { constexpr int j = J, cn = j >> 3, I = j & 7; if constexpr (cn < 2) dma_adv<I>(dB); else dma_adv<I>(dA); };
    auto vm_pre = [&](auto C_, auto V_) AI {
      constexpr int C = C_, v = V_, k = v - q_nvw(C) - q_nvx<NB>(C);
      if constexpr (v < q_nvw(C)) { if constexpr ((v & 3) == 0) dma_m0<v>(dQ); }
      else if constexpr (k >= 0) kv_pre(IC<q_kv0(C) + k>{});
    };
    auto vm_op = [&](auto C_, auto V_) AI {
      constexpr int C = C_, v = V_, xk = v - q_nvw(C), k = xk - q_nvx<NB>(C);
      if constexpr (v < q_nvw(C)) dma_ld<v, NTW>(dQ);
      else if constexpr (k < 0) xld(IC<(6 + 2 * C) * NB + xk>{});
      else kv_op(IC<q_kv0(C) + k>{});
    };
    auto vm_post = [&](auto C_, auto V_) AI {
      constexpr int C = C_, v = V_, k = v - q_nvw(C) - q_nvx<NB>(C);
      if constexpr (v < q_nvw(C)) dma_adv<v>(dQ);
      else if constexpr (k >= 0) kv_post(IC<q_kv0(C) + k>{});
    };
    constexpr int WT[7] = {8 + 2 * NB, 8 + 2 * NB, 8 + 2 * NB, 8 + 2 * NB, 8 + 2 * NB, 12, 12};
    PRE(2);
    wait_vm<WT[0]>();
    tie_v2(ep);
    tlo = rfl(ep[0]) + 1u; thi = rfl(ep[1]) + (tlo == 0u ? 1u : 0u);
    sfor<8 * NB>([&](auto K) AI { xbp(IC<0>{}, K); });
    asm volatile("s_waitcnt lgkmcnt(0)\n\ts_barrier" ::: "memory", XCLOB);
    POST(2); RT(2);
    sfor<PD>([&](auto F) AI { rdl(F); rdh(F); });
    sfor<7>([&](auto C_) AI {
      constexpr int C = C_;
      if constexpr (C <= 4) { dQ.mb = mb4 + QSL(C + 2); dQ.so = soQ + 64u * 8192u * (C + 2); }
      vm_pre(C_, IC<0>{});
      FENCE();
      sfor<32>([&](auto F_) AI {
        constexpr int f = F_, F = 32 * C + f, be = f >> 1, u = f & 1;
        constexpr bool nxt = C < 6 || f + PD < 32;
        if constexpr (f == FB && C < 6) {
          PRE(3 + C);
          asm volatile("s_waitcnt vmcnt(%0) lgkmcnt(0)\n\ts_barrier" :: "i"(WT[C + 1]) : "memory", XCLOB);
          POST(3 + C); RT(3 + C);
          FENCE();
        }
        if constexpr (qs.wait[F] >= 0) wait_lgkm<(qs.wait[F] > 15 ? 15 : qs.wait[F])>();
        auto mf = [&](auto Bk) AI { constexpr int blk = Bk, XR = XREG<NB>(2 * C + u, blk), RA = RING + 4 * (F % RG);
          if constexpr (C == 0 && u == 0) mf_xx0<RA, XR>(qa[blk][be]); else mf_xx<RA, XR>(qa[blk][be]);
          FENCE(); };
        auto wbn = [&]() AI { if constexpr (C < 6 && f >= 13 && ((f - 13) & 1) == 0 && (f - 13) / 2 < 8) {
          constexpr int j = (f - 13) / 2; vadd_ip<(int)(QSL(C + 1) - QSL(C))>(wb[j]); } };
        auto cv = [&](auto K) AI { constexpr int k = K, un = k >> 3, sp2 = un / NB, bb = un % NB; if constexpr (k >= 0 && sp2 < 8) qcv(IC<bb>{}, IC<sp2>{}, IC<(k & 7)>{}); };
        constexpr int v = q_vat<NB>(C, f), vn = q_vat<NB>(C, f + 1);
        constexpr int k0 = q_cv0<NB>(f);
        if constexpr (NB == 3) {
          mf(IC<0>{});
          if constexpr (v >= 0) vm_op(C_, IC<v>{});
          else if constexpr (q_bpa<NB>(C, f)) xbp(IC<C + 1>{}, IC<f / 2>{});
          else if constexpr (C == 6 && f >= 6) { cv(IC<k0>{}); cv(IC<k0 + 1>{}); }
          FENCE();
          mf(IC<1>{});
          if constexpr (nxt) rdl(IC<F + PD>{});
          FENCE();
          if constexpr (C < 6) wbn(); else if constexpr (f >= 6) cv(IC<k0 + (v >= 0 ? 0 : 2)>{});
          FENCE();
          mf(IC<2>{});
          if constexpr (nxt) rdh(IC<F + PD>{});
          if constexpr (q_bpb<NB>(C, f)) xbp(IC<C + 1>{}, IC<13 + f / 2>{});
          if constexpr (v >= 0) vm_post(C_, IC<v>{});
          if constexpr (vn >= 0) vm_pre(C_, IC<vn>{});
          FENCE();
        } else {
          mf(IC<0>{});
          if constexpr (nxt) rdl(IC<F + PD>{});
          if constexpr (q_bpa<NB>(C, f)) xbp(IC<C + 1>{}, IC<f>{});
          if constexpr (C == 6 && f >= 6) { cv(IC<k0>{}); cv(IC<k0 + 1>{}); }
          FENCE();
          wbn();
          FENCE();
          mf(IC<1>{});
          if constexpr (nxt) rdh(IC<F + PD>{});
          if constexpr (v >= 0) { vm_op(C_, IC<v>{}); vm_post(C_, IC<v>{}); }
          if constexpr (vn >= 0) vm_pre(C_, IC<vn>{});
          if constexpr (C == 6 && f >= 6) { cv(IC<k0 + 2>{}); cv(IC<k0 + 3>{}); }
          FENCE();
        }
      });
    });
    PRE(9);
    kv_pre(IC<20>{});
    asm volatile("s_nop 7" ::: "memory", XCLOB);
    FENCE();
    constexpr int DONE = q_cv0<NB>(32) / 8;
    constexpr int REST0 = q_cv0<NB>(32) % 8;
    sfor<8 - REST0>([&](auto K) AI { constexpr int un = DONE, sp2 = un / NB, bb = un % NB; if constexpr (REST0 != 0) { qcv(IC<bb>{}, IC<sp2>{}, IC<REST0 + K>{}); asm volatile("s_nop 1"); } });
    constexpr int U0 = DONE + (REST0 != 0 ? 1 : 0), NU = 8 * NB - U0;
    sfor<8>([&](auto Q_) AI { constexpr int q = Q_;
      if constexpr ((q & 1) == 0) { kv_op(IC<20 + q / 2>{}); kv_post(IC<20 + q / 2>{}); }
      sfor<NU>([&](auto U_) AI { constexpr int un = U0 + U_, sp2 = un / NB, bb = un % NB; qcv(IC<bb>{}, IC<sp2>{}, Q_); }); });
    sfor<NB - 1>([&](auto Bk) AI { sfor<8>([&](auto Sp) AI { c.Q[Bk + 1][Sp] = __builtin_bit_cast(bf16x8, qt[Bk + 1][Sp]); }); });
    sfor<32>([&](auto K) AI { acc_wr<QREG<NB>(0) + K>(qt[0][K / 4][K % 4]); });
    FENCE();
    asm volatile("s_nop 1" ::: "memory");
  }

  {
    dB.mb = mb4; dB.so = 2u * SLOT + 4096u * w;
    phaseA<NB, true, true>(c, 2u * SLOT, dB, pf, 10);
    PRE(11);
    phaseB0<NB>(c);
  }
  unsigned sl = 3u * SLOT, pv = 2u * SLOT, pv2 = SLOT;
  auto next = [&]() AI { pv2 = pv; pv = sl; sl = sl == 4u * SLOT ? 0u : sl + SLOT; };
  unsigned soA = SLOT + 4096u * w, soB = 3u * SLOT + 4096u * w, stepB = SLOT;
  for (int t = 1; t < 16; ++t) {
    if (t == 15) {
      dA.srd = srdWo; dA.inc = 6144u; dA.ginc = 96256u; soA = 28672u * w;
      const unsigned l = lane_id(), vWo = 896u * (l >> 3) + 16u * ((l & 7u) ^ (2u * ((l >> 4) & 3u)));
      sfor<4>([&](auto K) AI { dA.v[K] = mov_v(vWo); });
      const unsigned vo = (32u * (unsigned)b + 8u * w + (l >> 5)) * (DM * 2) + 384u + (l & 31u) * 16u;
      pf4_load<0>(vo, 0u, srdWo); pf4_load<2 * DM * 2>(vo, 0u, srdWo); pf4_load<0>(vo, 4u * DM * 2, srdWo); pf4_load<2 * DM * 2>(vo, 4u * DM * 2, srdWo);
    }
    dA.mb = mb4 + pv2; dA.so = soA; soA += SLOT;
    phaseA<NB, false, true>(c, sl, dA, pf, 10 + 2 * t);
    next();
    if (t >= 14) {
      dB.srd = dA.srd; dB.inc = dA.inc; dB.ginc = dA.ginc; soB = t == 14 ? 15u * SLOT + 4096u * w : 28672u * w + 128u;
      sfor<4>([&](auto K) AI { dB.v[K] = mov_v(dA.v[K]); });
    }
    dB.mb = mb4 + pv2; dB.so = soB; soB += SLOT;
    phaseB<NB, false, true>(c, sl, dB, pf, 11 + 2 * t);
    next();
  }
  dB.mb = mb4 + pv2; dB.so = 28672u * w + 256u;
  phaseB<NB, true, true>(c, sl, dB, pf, 42);
  next();
  PRE(43); wait_bar<16>(); POST(43);

  asm volatile("s_nop 7" ::: "memory");
  FENCE();
  Epi<NB> e;
  dB.mb = mb4 + pv2; dB.so = 28672u * w + 384u;
  dma_m0<0>(dB);
  sfor<NB>([&](auto Bk) AI { constexpr int blk = Bk;
    tie_v4(c.L[blk]);
    e.rl[blk] = __builtin_amdgcn_rcpf(c.L[blk][0]);
    sfor<8>([&](auto Sp) AI { constexpr int sp = Sp;
      constexpr int R = OREG(blk, 2 * sp);
      const float a0 = acc_rd<R>(), a1 = acc_rd<R + 1>(), a2 = acc_rd<R + 2>(), a3 = acc_rd<R + 3>();
      const float a4 = acc_rd<R + 4>(), a5 = acc_rd<R + 5>(), a6 = acc_rd<R + 6>(), a7 = acc_rd<R + 7>();
      u32x4 t = {cvt_v(a0, a1), cvt_v(a2, a3), cvt_v(a4, a5), cvt_v(a6, a7)};
      e.of[blk][sp] = __builtin_bit_cast(bf16x8, t);
      constexpr int pc = blk * 8 + sp;
      if constexpr (pc < 16) { FENCE(); dma_ld<(pc & 7), NTE>(dB); dma_adv<(pc & 7)>(dB);
        if constexpr (pc == 3 || pc == 11) dma_m0<4>(dB);
        if constexpr (pc == 7) { dB.mb = mb4 + pv; dB.so = 28672u * w + 512u; dma_m0<0>(dB); }
        FENCE(); } });
    sfor<2>([&](auto Nb) AI { e.y[1][blk][Nb] = f32x4{0.f, 0.f, 0.f, 0.f}; tie_v4(e.y[1][blk][Nb]); }); });
  FENCE();
  asm volatile("s_nop 1" ::: "memory");
  const unsigned laneE = lane_id(), gE = laneE >> 4, ccE = laneE & 15u, q4E = ccE >> 2;
  const unsigned obase = lds0 + 128u * (4u * gE + q4E) + 32u * ((2u * gE + (q4E >> 1)) & 3u) + 8u * (laneE & 3u);
  e.srdP = mk_srd(ws + (size_t)(grp * NH + h) * PARTB, PARTB);
  sfor<NB>([&](auto Bk) AI { e.vP[Bk] = rb[Bk] * (14u * 1024u) + laneE * 16u; });
  Red r;
  r.srdF = srdF; r.tlo = tlo; r.thi = thi; r.lane = laneE;
  r.vF = laneE == 0u ? (unsigned)h * 8u : OOB;
  r.srdR = mk_srd(ws + (size_t)grp * NH * PARTB, NH * PARTB);
  r.srdO = mk_srd((char*)out + ((size_t)b * SQ * 2 * DM + br * DM) * 2, (SQ * 2 * DM - br * DM) * 2);
  r.vR = (gE & 1u) * PARTB + (32u * (gE >> 1) + ccE) * 16u;
  r.vO = ccE * (2u * DM * 2u) + 32u * (gE & 1u) + 16u * (gE >> 1);

  asm volatile("; EPI_BEGIN" ::: XCLOB);
  u32x4 sv; sfor<4>([&](auto I) AI { constexpr int i = I; const unsigned m7 = ccE & 7u; const bool on = (gE >> 1) == (ccE >> 3);
    sv[i] = (on && m7 == 2u * i) ? 0x00003F80u : ((on && m7 == 2u * i + 1u) ? 0x3F800000u : 0u); });
  bf16x8 sel = __builtin_bit_cast(bf16x8, sv); tie_v8(sel);
  const unsigned c0 = (unsigned)h >> 2, ib = 16u + ((unsigned)h & 3u);
  const unsigned iw = w == 2u ? (unsigned)h : ib;
  const unsigned lw = w == 2u ? c0 : (ib >> 1) - 8u;
  POST(44); RT(44);
#pragma unroll 1
  for (int p = 0; p < 7; ++p) {
    const unsigned b0 = obase + sl;
    bf16x8 fr[4];
    yrd<NB, 0>(fr, b0); yrd<NB, 1>(fr, b0); yrd<NB, 2>(fr, b0);
    halfY<NB, 0>(e, fr, b0, p == 0 ? OOB : (unsigned)(2 * p - 1) * 1024u);
    if (p >= 1 && p <= 3) PRE(57 + p);
    if (p == 0) wait_vm<24 + NB>(); else if (p == 1) wait_vm<16 + 3 * NB>(); else if (p <= 3) wait_vm<2 * NB + 8>(); else wait_vm<2 * NB>();
    if (p >= 1 && p <= 3) PRE(60 + p);
    if (p >= 2) flag_set(r, (unsigned)(p - 2) * 4u + w);
    if (p == 1 || p == 2) { dB.mb = mb4 + pv; dB.so = 28672u * w + 128u * (unsigned)(4 + p); sfor<8>([&](auto I) AI { dma_piece<I, NTE>(dB); }); }
    if (p >= 1 && p <= 3) POST(57 + p);
    if constexpr (NB == 2) {
      if (p >= 3) {
        const unsigned cf = p - 5, cl = p - 4, cp = p - 3;
        if (p >= 5 && (w == 2u || c0 == cf)) red_finish<128>(r, sel, cf, iw);
        if (p >= 4 && (w == 2u || c0 == cl)) {
          if (!poll_ok(r)) flag_wait(r, cl * 4u + lw);
          red_issue<128>(r, cl, iw);
        }
        if (p <= 5 && (w == 2u || c0 == cp)) poll_issue(r, cp * 4u + lw);
      }
    }
    halfY<NB, 1>(e, fr, b0, (unsigned)(2 * p) * 1024u);
    pv = sl; sl = sl == 4u * SLOT ? 0u : sl + SLOT;
    PRE(45 + p);
    if (p == 3) wait_vm<3 * NB>();
    if (p < 4) asm volatile("s_barrier" ::: "memory");
    POST(45 + p); RT(45 + p);
  }
  {
    asm volatile("s_nop 7" ::: "memory");
    FENCE();
    u32x4 t[2];
    sfor<NB * 13>([&](auto K) AI { drain_op<NB, 1, K>(e, t, 13u * 1024u); });
  }
  wait_vm<2 * NB>();
  flag_set(r, 5u * 4u + w);
  wait_vm<1>();
  flag_set(r, 6u * 4u + w);
  POST(52); RT(52);
  if constexpr (NB == 2) {
    const unsigned ca = w + 1u, cb = w + 3u;
    flag_wait(r, ca * 4u + c0); red_issue<192>(r, ca, h);
    if (w == 2u || c0 == 2u) red_finish<128>(r, sel, 2u, iw);
    POST(55); RT(55);
    flag_wait(r, cb * 4u + c0); red_issue<128>(r, cb, h);
    POST(56); RT(56);
    if (w == 2u && h == 0) { u32x2 tg = {tlo, thi}; st8_sc1(tg, laneE == 0u ? 0u : OOB, 28u * 128u, srdF); }
    red_finish<192>(r, sel, ca, h);
    wait_vm<1>();
    red_finish<128>(r, sel, cb, h);
  } else {
    if (w == 0u) {
      const unsigned cB = c0 == 3u ? 3u : c0 + 4u;
      flag_wait(r, cB * 4u + (ib >> 1) - 8u); red_issue<128>(r, cB, ib);
      wait_vm<0>();
      red_finish<128>(r, sel, cB, ib);
    }
  }
}

__global__ __launch_bounds__(256, 1)
void attn_kernel(const __hip_bfloat16* __restrict__ xA, const __hip_bfloat16* __restrict__ kA, const __hip_bfloat16* __restrict__ vA,
                 const __hip_bfloat16* __restrict__ wqA, const __hip_bfloat16* __restrict__ woA,
                 const __hip_bfloat16* __restrict__ xB, const __hip_bfloat16* __restrict__ kB, const __hip_bfloat16* __restrict__ vB,
                 const __hip_bfloat16* __restrict__ wqB, const __hip_bfloat16* __restrict__ woB, char* __restrict__ ws, __hip_bfloat16* __restrict__ out) {
  extern __shared__ __attribute__((aligned(16))) char lds[];
  uint64_t pc0; asm volatile("s_getpc_b64 %0" : "=s"(pc0));
  const unsigned blk = blockIdx.x, xcd = blk & 7u, j = blk >> 3;
  const int br = xcd & 1u, h = (xcd >> 1) * 4 + (j & 3u), b = j >> 2;
  const unsigned lane = threadIdx.x & 63u, w = rfl(threadIdx.x >> 6);
  const unsigned lds0 = (unsigned)(size_t)lds;
  const __hip_bfloat16* x  = br ? xB : xA;
  const __hip_bfloat16* kc = br ? kB : kA;
  const __hip_bfloat16* vc = br ? vB : vA;
  const __hip_bfloat16* wq = br ? wqB : wqA;
  const __hip_bfloat16* wo = br ? woB : woA;
  if (w < 2) body<3>(x, kc, vc, wq, wo, ws, out, br, b, h, j, w, lane, lds0, pc0);
  else       body<2>(x, kc, vc, wq, wo, ws, out, br, b, h, j, w, lane, lds0, pc0);
}

extern "C" void kernel_launch(void* const* d_in, const int* in_sizes, int n_in, void* d_out, int out_size, void* d_ws, size_t ws_size,
                              hipStream_t stream) {
  (void)in_sizes; (void)n_in; (void)out_size; (void)ws_size;
  typedef const __hip_bfloat16* P;
  (void)hipFuncSetAttribute((const void*)attn_kernel, hipFuncAttributeMaxDynamicSharedMemorySize, 163840);
  attn_kernel<<<256, 256, 163840, stream>>>((P)d_in[0], (P)d_in[1], (P)d_in[2], (P)d_in[3], (P)d_in[4],
                                            (P)d_in[5], (P)d_in[6], (P)d_in[7], (P)d_in[8], (P)d_in[9], (char*)d_ws, (__hip_bfloat16*)d_out);
}
```

```cpp
#include <hip/hip_runtime.h>
#include <hip/hip_bf16.h>
#include <utility>
#include <stdint.h>

#define NTX ""
#define NTP ""
#define NTW false
#define NTE true
#define NTR " nt"
#define NTS ""
typedef float    f32x4  __attribute__((ext_vector_type(4)));
typedef float    f32x2  __attribute__((ext_vector_type(2)));
typedef __bf16   bf16x8 __attribute__((ext_vector_type(8)));
typedef __bf16   bf16x4 __attribute__((ext_vector_type(4)));
typedef __bf16   bf16x2 __attribute__((ext_vector_type(2)));
typedef unsigned u32x4  __attribute__((ext_vector_type(4)));
typedef unsigned u32x2  __attribute__((ext_vector_type(2)));

#define DI __device__ __forceinline__
#define LDSP __attribute__((address_space(3)))
#define FENCE() __builtin_amdgcn_sched_barrier(0)
#define AI __attribute__((always_inline))

template <class F, int... Is> DI void sfor_(F&& f, std::integer_sequence<int, Is...>) { (f(std::integral_constant<int, Is>{}), ...); }
template <int N, class F> DI void sfor(F&& f) { sfor_(f, std::make_integer_sequence<int, N>{}); }
template <int V> using IC = std::integral_constant<int, V>;

constexpr int SQ = 160, DM = 448, NH = 16, SK = 1024, DH = 256;
constexpr unsigned SLOT = 32768u;
constexpr float SCL = 0.09016844005556021f;
constexpr float THR = 8.0f;
constexpr unsigned SRD3 = 0x00020000u;
constexpr unsigned PARTB = 140u * 1024u;
constexpr size_t FLAG_OFF = 40u << 20;
constexpr unsigned OOB = 0x40000000u;
constexpr unsigned CODE_PF = 88u * 1024u;

constexpr unsigned QSL(int C) { return (C % 3 == 0 ? 1u : (C % 3 == 1 ? 4u : 0u)) * SLOT; }

DI unsigned lane_id() { unsigned l; asm volatile("v_mbcnt_lo_u32_b32 %0, -1, 0\n\tv_mbcnt_hi_u32_b32 %0, -1, %0" : "=v"(l)); return l; }
DI unsigned rfl(unsigned x) { return __builtin_amdgcn_readfirstlane(x); }
DI u32x4 mk_srd(const void* p, unsigned bytes) {
  const uint64_t a = (uint64_t)p;
  u32x4 r; r[0] = rfl((unsigned)a); r[1] = rfl((unsigned)(a >> 32)); r[2] = bytes; r[3] = SRD3; return r;
}
DI unsigned cvtpk(float lo, float hi) { f32x2 v = {lo, hi}; bf16x2 b = __builtin_convertvector(v, bf16x2); return __builtin_bit_cast(unsigned, b); }
DI float max3f(float a, float b, float c) { float r; asm("v_max3_f32 %0, %1, %2, %3" : "=v"(r) : "v"(a), "v"(b), "v"(c)); return r; }
DI float max2f(float a, float b) { float r; asm("v_max_f32_e32 %0, %1, %2" : "=v"(r) : "v"(a), "v"(b)); return r; }
DI float fma_s(float a, float b, float c) { float r; asm("v_fma_f32 %0, %1, %2, %3" : "=v"(r) : "v"(a), "s"(b), "v"(c)); return r; }
DI void fma_ip(float& x, float s, float c) { asm volatile("v_fma_f32 %0, %0, %1, %2" : "+v"(x) : "s"(s), "v"(c)); }
DI void exp_ip(float& x) { asm volatile("v_exp_f32_e32 %0, %0" : "+v"(x)); }
DI unsigned cvt_v(float lo, float hi) { unsigned r; asm volatile("v_cvt_pk_bf16_f32 %0, %1, %2" : "=v"(r) : "v"(lo), "v"(hi)); return r; }
DI float mul_v(float a, float b) { float r; asm volatile("v_mul_f32_e32 %0, %1, %2" : "=v"(r) : "v"(a), "v"(b)); return r; }

DI void mf_vv(f32x4& d, const bf16x8& a, const bf16x8& b) { asm volatile("v_mfma_f32_16x16x32_bf16 %0, %1, %2, %0" : "+v"(d) : "v"(a), "v"(b)); }
DI void mf_vv0(f32x4& d, const bf16x8& a, const bf16x8& b) { asm volatile("v_mfma_f32_16x16x32_bf16 %0, %1, %2, 0" : "=&v"(d) : "v"(a), "v"(b)); }

template <int MO, bool NT> DI unsigned dma(unsigned mb, unsigned so, unsigned step, unsigned voff, const u32x4& srd) {
  unsigned nso;
  if constexpr (NT) asm volatile("s_add_u32 m0, %1, %2\n\ts_add_u32 %0, %3, %4\n\tbuffer_load_dwordx4 %5, %6, %3 offen nt lds" : "=&s"(nso) : "s"(mb), "i"(MO), "s"(so), "s"(step), "v"(voff), "s"(srd) : "scc");
  else              asm volatile("s_add_u32 m0, %1, %2\n\ts_add_u32 %0, %3, %4\n\tbuffer_load_dwordx4 %5, %6, %3 offen lds"    : "=&s"(nso) : "s"(mb), "i"(MO), "s"(so), "s"(step), "v"(voff), "s"(srd) : "scc");
  return nso;
}
DI bf16x8 lds_b128(unsigned addr) { return *(const LDSP bf16x8*)addr; }
DI bf16x8 lds_tr2(unsigned addr0, unsigned addr1) {
  bf16x4 lo = __builtin_amdgcn_ds_read_tr16_b64_v4bf16((LDSP bf16x4*)addr0);
  bf16x4 hi = __builtin_amdgcn_ds_read_tr16_b64_v4bf16((LDSP bf16x4*)addr1);
  return __builtin_shufflevector(lo, hi, 0, 1, 2, 3, 4, 5, 6, 7);
}
DI bf16x4 lds_tr(unsigned addr) { return __builtin_amdgcn_ds_read_tr16_b64_v4bf16((LDSP bf16x4*)addr); }
DI bf16x8 cat8(const bf16x4& lo, const bf16x4& hi) { return __builtin_shufflevector(lo, hi, 0, 1, 2, 3, 4, 5, 6, 7); }
DI void pf_load(unsigned& dummy, unsigned vo, const u32x4& srd) { asm volatile("buffer_load_dword %0, %1, %2, 0 offen" : "+v"(dummy) : "v"(vo), "s"(srd)); }
DI void ld8_sc1(u32x2& d, unsigned vo, const u32x4& srd) { asm volatile("buffer_load_dwordx2 %0, %1, %2, 0 offen sc1" : "=v"(d) : "v"(vo), "s"(srd)); }
DI void st8_sc1(const u32x2& d, unsigned vo, unsigned so, const u32x4& srd) { asm volatile("buffer_store_dwordx2 %0, %1, %2, %3 offen sc1" :: "v"(d), "v"(vo), "s"(srd), "s"(so) : "memory"); }
DI void st16_sc1(const u32x4& d, unsigned vo, unsigned so, const u32x4& srd) { asm volatile("buffer_store_dwordx4 %0, %1, %2, %3 offen sc1" NTS :: "v"(d), "v"(vo), "s"(srd), "s"(so) : "memory"); }
#define OUTPOL " sc1"
DI void st16(const u32x4& d, unsigned vo, unsigned so, const u32x4& srd) { asm volatile("buffer_store_dwordx4 %0, %1, %2, %3 offen" OUTPOL :: "v"(d), "v"(vo), "s"(srd), "s"(so) : "memory"); }
#define XCLOB "a0","a1","a2","a3","a4","a5","a6","a7","a8","a9","a10","a11","a12","a13","a14","a15","a16","a17","a18","a19","a20","a21","a22","a23", \
  "a24","a25","a26","a27","a28","a29","a30","a31","a32","a33","a34","a35","a36","a37","a38","a39","a40","a41","a42","a43","a44","a45","a46","a47", \
  "a48","a49","a50","a51","a52","a53","a54","a55","a56","a57","a58","a59","a60","a61","a62","a63","a64","a65","a66","a67","a68","a69","a70","a71", \
  "a72","a73","a74","a75","a76","a77","a78","a79","a80","a81","a82","a83","a84","a85","a86","a87","a88","a89","a90","a91","a92","a93","a94","a95", \
  "a96","a97","a98","a99","a100","a101","a102","a103","a104","a105","a106","a107","a108","a109","a110","a111","a112","a113","a114","a115","a116","a117","a118","a119", \
  "a120","a121","a122","a123","a124","a125","a126","a127","a128","a129","a130","a131","a132","a133","a134","a135","a136","a137","a138","a139","a140","a141","a142","a143", \
  "a144","a145","a146","a147","a148","a149","a150","a151","a152","a153","a154","a155","a156","a157","a158","a159","a160","a161","a162","a163","a164","a165","a166","a167", \
  "a168","a169","a170","a171","a172","a173","a174","a175","a176","a177","a178","a179","a180","a181","a182","a183","a184","a185","a186","a187","a188","a189","a190","a191", \
  "a192","a193","a194","a195","a196","a197","a198","a199","a200","a201","a202","a203","a204","a205","a206","a207","a208","a209","a210","a211","a212","a213","a214","a215", \
  "a216","a217","a218","a219","a220","a221","a222","a223","a224","a225","a226","a227","a228","a229","a230","a231","a232","a233","a234","a235","a236","a237","a238","a239", \
  "a240","a241","a242","a243","a244","a245","a246","a247","a248","a249","a250","a251","a252","a253","a254","a255"
template <int R, int OFF> DI void xa_load(unsigned vo, const u32x4& srd) {
  asm volatile("buffer_load_dwordx4 a[%c0:%c1], %2, %3, 0 offen offset:%4" NTX :: "i"(R), "i"(R + 3), "v"(vo), "s"(srd), "i"(OFF) : XCLOB); }
template <int R> DI void xa_perm(unsigned pa) { asm volatile("ds_bpermute_b32 a%c0, %1, a%c0" :: "i"(R), "v"(pa) : XCLOB); }
template <int R> DI void mf_vx(f32x4& d, const bf16x8& a) { asm volatile("v_mfma_f32_16x16x32_bf16 %0, %1, a[%c2:%c3], %0" : "+v"(d) : "v"(a), "i"(R), "i"(R + 3)); }
template <int R> DI void mf_vx0(f32x4& d, const bf16x8& a) { asm volatile("v_mfma_f32_16x16x32_bf16 %0, %1, a[%c2:%c3], 0" : "=&v"(d) : "v"(a), "i"(R), "i"(R + 3)); }
template <int NB> constexpr int XREG(int ks, int blk) { return NB == 3 ? (ks < 10 ? 12 * ks : (ks < 12 ? 192 + 12 * (ks - 10) : 224 + 12 * (ks - 12))) + 4 * blk : 8 * ks + 4 * blk; }
constexpr int OREG(int blk, int db) { return 64 * blk + 4 * db; }
template <int NB> constexpr int QREG(int sp) { return (NB == 3 ? 192 : 128) + 4 * sp; }
template <int R> DI void mf_o(const bf16x8& a, const bf16x8& b) { asm volatile("v_mfma_f32_16x16x32_bf16 a[%c2:%c3], %0, %1, a[%c2:%c3]" :: "v"(a), "v"(b), "i"(R), "i"(R + 3)); }
template <int R> DI void acc_zero() { asm volatile("v_accvgpr_write_b32 a%c0, 0" :: "i"(R)); }
template <int R> DI void acc_wr(unsigned v) { asm volatile("v_accvgpr_write_b32 a%c1, %0" :: "v"(v), "i"(R)); }
template <int R> DI float acc_rd() { float r; asm volatile("v_accvgpr_read_b32 %0, a%c1" : "=v"(r) : "i"(R)); return r; }
template <int R> DI void acc_mul(float f) { float t; asm volatile("v_accvgpr_read_b32 %0, a%c2\n\tv_mul_f32_e32 %0, %1, %0\n\tv_accvgpr_write_b32 a%c2, %0" : "=&v"(t) : "v"(f), "i"(R)); }
DI void pfa_load(unsigned vo, unsigned so, const u32x4& srd) { asm volatile("buffer_load_dword a255, %0, %1, %2 offen" :: "v"(vo), "s"(srd), "s"(so)); }
template <int OFF> DI void pf4_load(unsigned vo, unsigned so, const u32x4& srd) {
  asm volatile("buffer_load_dwordx4 a[252:255], %0, %1, %2 offen offset:%3" NTP :: "v"(vo), "s"(srd), "s"(so), "i"(OFF)); }
template <int OFF> DI void pf4_keep(unsigned vo, unsigned so, const u32x4& srd) {
  asm volatile("buffer_load_dwordx4 a[252:255], %0, %1, %2 offen offset:%3" :: "v"(vo), "s"(srd), "s"(so), "i"(OFF)); }
template <int IMM> DI void vadd_ip(unsigned& x) { asm volatile("v_add_u32 %0, %1, %0" : "+v"(x) : "i"(IMM)); }
template <int IMM> DI void sadd_ip(unsigned& x) { asm volatile("s_add_u32 %0, %0, %1" : "+s"(x) : "i"(IMM) : "scc"); }
template <int IMM> DI void m0_set(unsigned mb) { asm volatile("s_add_u32 m0, %0, %1" :: "s"(mb), "i"(IMM) : "scc"); }
template <int GO, int OFF> DI void dma_nt(unsigned vo, unsigned sbase, const u32x4& srd) {
  unsigned so; asm volatile("s_add_u32 %0, %2, %3\n\tbuffer_load_dwordx4 %1, %4, %0 offen offset:%5 nt lds" : "=&s"(so) : "v"(vo), "s"(sbase), "i"(GO), "s"(srd), "i"(OFF) : "scc"); }
template <int R, int OFF> DI void tr_acc(unsigned addr) { asm volatile("ds_read_b64_tr_b16 a[%c0:%c1], %2 offset:%3" :: "i"(R), "i"(R + 1), "v"(addr), "i"(OFF)); }
template <int RA, int RB> DI void mf_xx(f32x4& d) { asm volatile("v_mfma_f32_16x16x32_bf16 %0, a[%c1:%c2], a[%c3:%c4], %0" : "+v"(d) : "i"(RA), "i"(RA + 3), "i"(RB), "i"(RB + 3)); }
template <int RA, int RB> DI void mf_xx0(f32x4& d) { asm volatile("v_mfma_f32_16x16x32_bf16 %0, a[%c1:%c2], a[%c3:%c4], 0" : "=&v"(d) : "i"(RA), "i"(RA + 3), "i"(RB), "i"(RB + 3)); }
template <int N> DI void wait_lgkm() { asm volatile("s_waitcnt lgkmcnt(%0)" :: "i"(N)); }
constexpr int QPD = 5, QFB = 32 - QPD;
template <int NB> constexpr bool q_bpa(int C, int f) { return NB == 3 ? (C < 6 && (f & 1) == 1 && f / 2 < 13) : (C < 6 && f < 16); }
template <int NB> constexpr bool q_bpb(int C, int f) { return NB == 3 && C < 6 && (f & 1) == 0 && f / 2 < 11; }
constexpr int q_nvw(int C) { return C <= 4 ? 8 : 0; }
template <int NB> constexpr int q_nvx(int C) { return C <= 3 ? 2 * NB : 0; }
constexpr int q_kv0(int C) { return C == 4 ? 0 : (C == 5 ? 4 : 12); }
constexpr int q_nvk(int C) { return C < 4 ? 0 : (C == 4 ? 4 : 8); }
template <int NB> constexpr int q_vat(int C, int f) {
  const int nv = q_nvw(C) + q_nvx<NB>(C) + q_nvk(C);
  if (f >= QFB || (f & 1)) return -1;
  if (nv > 8) return f / 2 < nv ? f / 2 : -1;
  constexpr int p8[8] = {0, 4, 8, 10, 14, 18, 22, 26};
  for (int v = 0; v < nv; ++v) if (p8[v] == f) return v;
  return -1;
}
template <int NB> constexpr int q_cv0(int f) {
  int k = 0;
  for (int g = 6; g < f; ++g) k += NB == 3 ? (q_vat<NB>(6, g) >= 0 ? 1 : 3) : 4;
  return k;
}
template <int NB> struct QSched {
  int wait[224];
  constexpr QSched() : wait{} {
    int pos[224 + QPD] = {}; int n = 0, bar = 0;
    for (int F = 0; F < QPD; ++F) { n += 2; pos[F] = n; }
    for (int F = 0; F < 224; ++F) {
      const int C = F / 32, f = F % 32;
      if (f == QFB && C < 6) bar = n;
      wait[F] = pos[F] <= bar ? -1 : n - pos[F];
      const bool nxt = F + QPD < 224;
      if (NB == 3) { n += q_bpa<NB>(C, f); if (nxt) n += 2; pos[F + QPD] = n; n += q_bpb<NB>(C, f); }
      else { if (nxt) n += 1; n += q_bpa<NB>(C, f); if (nxt) n += 1; pos[F + QPD] = n; }
    }
  }
};
DI unsigned mov_v(unsigned a) { unsigned r; asm volatile("v_mov_b32 %0, %1" : "=v"(r) : "v"(a)); return r; }
DI void swap32(unsigned& a, unsigned& b) { asm volatile("v_permlane32_swap_b32 %0, %1" : "+v"(a), "+v"(b)); }
DI void swap16(unsigned& a, unsigned& b) { asm volatile("v_permlane16_swap_b32 %0, %1" : "+v"(a), "+v"(b)); }
DI void tie_v8(bf16x8& d) { asm volatile("" : "+v"(d)); }
DI void tie_v4(f32x4& d) { asm volatile("" : "+v"(d)); }
DI void tie_v4u(u32x4& d) { asm volatile("" : "+v"(d)); }
DI void tie_v2(u32x2& d) { asm volatile("" : "+v"(d)); }
DI void tie_v(unsigned& d) { asm volatile("" : "+v"(d)); }
template <int N> DI void wait_vm() { asm volatile("s_waitcnt vmcnt(%0)" :: "i"(N) : "memory"); }
template <int N> DI void wait_bar() { asm volatile("s_waitcnt vmcnt(%0) lgkmcnt(0)\n\ts_barrier" :: "i"(N) : "memory", XCLOB); }

#define RTL(i)
struct Prof {};
#define PRE(i)
#define POST(i)
#define RT(i)

struct Desc { u32x4 srd; unsigned so, inc, ginc, mb; unsigned v[4]; };

template <int NB> struct Ctx {
  f32x4  L[NB];
  bf16x8 Q[NB][8];
  f32x4  S[NB][4];
  u32x4  P[NB][2];
  float  nm[NB], thr[NB], dl[NB], mx[NB];
  bf16x8 ones;
  unsigned kbase, vbase;
};

template <int I> DI void dma_m0(const Desc& d) { asm volatile("s_add_u32 m0, %0, %1" :: "s"(d.mb), "i"(16384 * (I >> 2)) : "scc"); }
template <int I, bool NT> DI void dma_ld(const Desc& d) {
  if constexpr (NT) asm volatile("buffer_load_dwordx4 %0, %1, %2 offen offset:%3 nt lds" :: "v"(d.v[I & 3]), "s"(d.srd), "s"(d.so), "i"(1024 * (I & 3)));
  else              asm volatile("buffer_load_dwordx4 %0, %1, %2 offen offset:%3 lds"    :: "v"(d.v[I & 3]), "s"(d.srd), "s"(d.so), "i"(1024 * (I & 3)));
}
template <int I> DI void dma_adv(Desc& d) { if constexpr (I < 7) asm volatile("s_add_u32 %0, %0, %1" : "+s"(d.so) : "s"((I & 3) == 3 ? d.ginc : d.inc) : "scc"); }
template <int I, bool NT> DI void dma_piece(Desc& d) {
  if constexpr ((I & 3) == 0) { dma_m0<I>(d); asm volatile("s_nop 0"); }
  dma_ld<I, NT>(d); dma_adv<I>(d);
}

template <int NB, int KB0, int K> DI void smF(Ctx<NB>& c) { constexpr int kb = KB0 + K / (4 * NB), blk = (K % (4 * NB)) / 4, r = K % 4;
  float x = c.S[blk][kb][r]; fma_ip(x, SCL, c.nm[blk]); c.S[blk][kb][r] = x; }
template <int NB, int KB0, int K> DI void smE(Ctx<NB>& c) { constexpr int kb = KB0 + K / (4 * NB), blk = (K % (4 * NB)) / 4, r = K % 4;
  float x = c.S[blk][kb][r]; exp_ip(x); c.S[blk][kb][r] = x; }
template <int NB, int KB0, int J> DI void smC(Ctx<NB>& c) { constexpr int ko = J / (2 * NB), blk = (J % (2 * NB)) / 2, pr = J % 2;
  c.P[blk][KB0 >> 1][2 * ko + pr] = cvt_v(c.S[blk][KB0 + ko][2 * pr], c.S[blk][KB0 + ko][2 * pr + 1]); }
template <int NB, int KB0, int K> DI void sm_gen(Ctx<NB>& c) {
  constexpr int j = K / 5, pos = K % 5;
  if constexpr (pos < 2) { if constexpr (j < 2 * NB * 2) smF<NB, KB0, 2 * j + pos>(c); }
  else if constexpr (pos < 4) { if constexpr (j < 2 * NB * 2) smE<NB, KB0, 2 * j + pos - 2>(c); }
  else if constexpr (j >= 1 && j - 1 < 2 * NB * 2) smC<NB, KB0, j - 1>(c);
}

DI float max3v(float a, float b, float c) { float r; asm volatile("v_max3_f32 %0, %1, %2, %3" : "=v"(r) : "v"(a), "v"(b), "v"(c)); return r; }
template <int NB, int J> DI void smM(Ctx<NB>& c) { constexpr int kb = J / (2 * NB), hf = (J % (2 * NB)) / NB, blk = J % NB;
  if constexpr (kb == 0 && hf == 0) c.mx[blk] = max3v(c.S[blk][0][0], c.S[blk][0][1], c.S[blk][0][1]);
  else c.mx[blk] = max3v(c.mx[blk], c.S[blk][kb][2 * hf], c.S[blk][kb][2 * hf + 1]); }

template <int NB, bool FIRST, bool NT> DI void phaseA(Ctx<NB>& c, unsigned slot, Desc& d, Prof& pf, int si) {
  PRE(si); wait_bar<16>(); POST(si); RTL(si);
  const unsigned b0 = c.kbase + slot;
  unsigned kb4[4] = {b0, b0 ^ 64u, b0 ^ 128u, b0 ^ 192u};
#define PDA3 4
#define PDA2 3
  constexpr int PD = NB == 3 ? PDA3 : PDA2, RG = PD + 1;
  bf16x8 fr[RG];
  auto rd = [&](auto F) AI { constexpr int f = F, kb = f >> 3, s = f & 7; fr[f % RG] = lds_b128(kb4[s & 3] + 256 * (s >> 2) + 8192 * kb); };
  sfor<PD>(rd);
  dma_m0<0>(d);
  FENCE();
  auto oz = [&](auto Z) AI { constexpr int z = Z;
    if constexpr (z < NB * 64) acc_zero<z>(); };
  auto mf = [&](auto F, auto Bk) AI {
    constexpr int f = F, kb = f >> 3, s = f & 7, blk = Bk;
    if constexpr (blk == 0) { if constexpr (s == 0) mf_vx0<QREG<NB>(s)>(c.S[0][kb], fr[f % RG]); else mf_vx<QREG<NB>(s)>(c.S[0][kb], fr[f % RG]); }
    else                    { if constexpr (s == 0) mf_vv0(c.S[blk][kb], fr[f % RG], c.Q[blk][s]); else mf_vv(c.S[blk][kb], fr[f % RG], c.Q[blk][s]); }
    FENCE();
  };
  auto dsal = [&](auto F) AI { constexpr int f = F;
    if constexpr ((f & 3) == 2) dma_adv<(f >> 2)>(d);
    if constexpr (f == 16) dma_m0<4>(d); };
  if constexpr (NB == 3) {
    if constexpr (!FIRST) {
      sfor<6>([&](auto K) AI { smF<3, 2, K>(c); });
      sfor<4>([&](auto K) AI { smE<3, 2, K>(c); });
      smC<3, 2, 0>(c);
      FENCE();
    }
    sfor<32>([&](auto F) AI {
      constexpr int f = F;
      mf(F, IC<0>{});
      if constexpr (FIRST) { oz(IC<6 * f>{}); oz(IC<6 * f + 1>{}); }
      else if constexpr (f < 20) smE<3, 2, f + 4>(c);
      else if constexpr (f < 23) { smM<3, 6 + 2 * (f - 20)>(c); smM<3, 7 + 2 * (f - 20)>(c); }
      else if constexpr (f >= 26 && f < 29) { smM<3, 12 + 2 * (f - 26)>(c); smM<3, 13 + 2 * (f - 26)>(c); }
      FENCE();
      mf(F, IC<1>{});
      if constexpr (f + PD < 32) rd(IC<f + PD>{});
      FENCE();
      if constexpr (FIRST) { oz(IC<6 * f + 2>{}); oz(IC<6 * f + 3>{}); }
      else if constexpr (f + 6 < 24) smF<3, 2, f + 6>(c);
      FENCE();
      mf(F, IC<2>{});
      if constexpr ((f & 3) == 1) dma_ld<(f >> 2), NT>(d);
      if constexpr (FIRST) { oz(IC<6 * f + 4>{}); oz(IC<6 * f + 5>{}); }
      else if constexpr (!(f & 1) && f / 2 + 1 < 12) smC<3, 2, f / 2 + 1>(c);
      else if constexpr (f == 11 || f == 15 || f == 19) { smM<3, 2 * ((f - 11) / 4)>(c); smM<3, 2 * ((f - 11) / 4) + 1>(c); }
      dsal(F);
      FENCE();
    });
  } else {
    constexpr int NOPS = NB * 4 * 5 + 5, GF = 22 * NB;
    sfor<32>([&](auto F) AI {
      constexpr int f = F;
      sfor<NB>([&](auto Bk) AI {
        constexpr int blk = Bk, g = f * NB + blk;
        mf(F, Bk);
        if constexpr (blk == 0 && f + PD < 32) rd(IC<f + PD>{});
        if constexpr (blk == 1 && (f & 3) == 1) dma_ld<(f >> 2), NT>(d);
        FENCE();
        if constexpr (FIRST) { oz(IC<2 * g>{}); oz(IC<2 * g + 1>{}); }
        else {
          constexpr int lo = (g < GF) ? (g * NOPS) / GF : NOPS, hi = (g + 1 < GF) ? ((g + 1) * NOPS) / GF : NOPS;
          sfor<hi - lo>([&](auto J) AI { sm_gen<NB, 2, lo + J>(c); });
          if constexpr (g >= GF && g < GF + 4 * NB) smM<NB, g - GF>(c);
          if constexpr (g >= 26 * NB && g < 28 * NB) smM<NB, 4 * NB + g - 26 * NB>(c);
        }
        if constexpr (blk == 1) dsal(F);
        FENCE();
      });
    });
  }
}

template <int NB> DI float rowmax16(const f32x4* s) {
  float m = max3f(s[0][0], s[0][1], s[0][2]);
  m = max3f(m, s[0][3], s[1][0]); m = max3f(m, s[1][1], s[1][2]); m = max3f(m, s[1][3], s[2][0]);
  m = max3f(m, s[2][1], s[2][2]); m = max3f(m, s[2][3], s[3][0]); m = max3f(m, s[3][1], s[3][2]);
  return max2f(m, s[3][3]);
}
DI float xl32(float v) { auto p = __builtin_amdgcn_permlane32_swap(__float_as_uint(v), __float_as_uint(v), false, false); return max2f(__uint_as_float(p[0]), __uint_as_float(p[1])); }
DI float xl16(float v) { auto p = __builtin_amdgcn_permlane16_swap(__float_as_uint(v), __float_as_uint(v), false, false); return max2f(__uint_as_float(p[0]), __uint_as_float(p[1])); }
constexpr float THR_RAW = THR / SCL;

template <int NB> DI void phaseB0(Ctx<NB>& c) {
  asm volatile("s_nop 7" ::: "memory");
  FENCE();
  sfor<NB>([&](auto Bk) AI { constexpr int blk = Bk; c.mx[blk] = rowmax16<NB>(c.S[blk]); });
  sfor<NB>([&](auto Bk) AI { constexpr int blk = Bk; const float m = xl16(xl32(c.mx[blk])); c.nm[blk] = -SCL * m; c.thr[blk] = m + THR_RAW; c.dl[blk] = 0.f;
    c.L[blk] = f32x4{0.f, 0.f, 0.f, 0.f}; });
  sfor<NB * 20 + 5>([&](auto J) AI { sm_gen<NB, 0, J>(c); });
}

#define BDMA 1
template <int NB, bool FINAL, bool NT> DI void phaseB(Ctx<NB>& c, unsigned slot, Desc& d, Prof& pf, int si) {
  PRE(si); wait_bar<16>(); POST(si); RTL(si);
  const unsigned b0 = c.vbase + slot;
#define PDB3 4
#define PDB2 3
  constexpr int PD = NB == 3 ? PDB3 : PDB2, RG = PD + 1;
  bf16x4 flo[RG], fhi[RG];
  unsigned vb[8];
  auto rdl = [&](auto F) AI { constexpr int f = F, u = f >> 4, j = (f >> 1) & 7, hi = f & 1; flo[f % RG] = lds_tr(vb[j] + 256 * hi + 16384 * u); };
  auto rdh = [&](auto F) AI { constexpr int f = F, u = f >> 4, j = (f >> 1) & 7, hi = f & 1; fhi[f % RG] = lds_tr(vb[j] + 256 * hi + 16384 * u + 8192); };
  vb[0] = b0; vb[1] = b0 ^ 32u; vb[2] = b0 ^ 64u; vb[3] = b0 ^ 96u;
  sfor<PD>([&](auto F) AI { rdl(F); rdh(F); });
  asm volatile("" ::: "memory");
  FENCE();
  dma_m0<0>(d);
  sfor<6>([&](auto J) AI { if constexpr (2 * (2 + J) >= PD) { vb[2 + J] = b0 ^ (32u * (2 + J)); tie_v(vb[2 + J]); } });
  unsigned long long chg = 0;
  if constexpr (!FINAL) {
    sfor<2 * NB>([&](auto J) AI { smM<NB, 6 * NB + J>(c); });
    sfor<NB>([&](auto Bk) AI { constexpr int blk = Bk; chg |= __ballot(c.mx[blk] > c.thr[blk]); });
    if (__builtin_expect(chg != 0ull, 0)) {
      sfor<NB>([&](auto Bk) AI { constexpr int blk = Bk;
        const float m = xl16(xl32(c.mx[blk]));
        const float t = fma_s(m, SCL, c.nm[blk]);
        const bool mv = m > c.thr[blk];
        c.dl[blk] = mv ? t : 0.f;
        c.nm[blk] -= c.dl[blk];
        c.thr[blk] = mv ? m + THR_RAW : c.thr[blk]; });
    }
  }
  FENCE();
  auto mfl = [&](auto U, auto Bk) AI { mf_vv(c.L[Bk], c.ones, __builtin_bit_cast(bf16x8, c.P[Bk][U])); FENCE(); };
  auto mfo = [&](auto F, auto Bk) AI { constexpr int f = F, u = f >> 4, db = ((f >> 1) & 7) + 8 * (f & 1);
    mf_o<OREG(Bk, db)>(cat8(flo[f % RG], fhi[f % RG]), __builtin_bit_cast(bf16x8, c.P[Bk][u])); FENCE(); };
  auto dsal = [&](auto F) AI { constexpr int f = F;
    if constexpr ((f & 3) == 2) dma_adv<(f >> 2)>(d);
    if constexpr (f == 16) dma_m0<4>(d); };
  if constexpr (NB == 3 && BDMA == 0) {
    if constexpr (FINAL) {
      sfor<20>([&](auto K) AI { smF<3, 2, K>(c); });
      sfor<9>([&](auto K) AI { smE<3, 2, K>(c); });
      FENCE();
    }
    sfor<2>([&](auto U_) AI {
      constexpr int u = U_;
      sfor<3>([&](auto Bk) AI {
        constexpr int blk = Bk;
        mfl(U_, Bk);
        if constexpr (!FINAL) {
          if constexpr (u == 0) { smF<3, 0, 2 * blk>(c); smF<3, 0, 2 * blk + 1>(c); } else smE<3, 0, 12 + blk>(c);
        } else if constexpr (u == 0) smE<3, 2, 9 + blk>(c);
        FENCE();
      });
      sfor<16>([&](auto FF) AI {
        constexpr int f = 16 * u + FF, nd = f - ((f + 2) >> 2);
        mfo(IC<f>{}, IC<0>{});
        if constexpr ((f & 3) == 1) dma_ld<(f >> 2), NT>(d);
        else if constexpr (!FINAL) { constexpr int e = u == 0 ? nd : nd + 3; if constexpr (e < 24) smE<3, 0, e>(c); }
        else if constexpr (u == 0) smE<3, 2, 12 + nd>(c);
        FENCE();
        mfo(IC<f>{}, IC<1>{});
        if constexpr (f + PD < 32) rdl(IC<f + PD>{});
        FENCE();
        if constexpr (!FINAL) {
          if constexpr (f < 16) smF<3, 0, f + 6>(c);
          else if constexpr (f < 22) smC<3, 0, f - 16>(c);
          else if constexpr (f < 24) smF<3, 0, f>(c);
          else if constexpr (f < 30) smC<3, 0, f - 18>(c);
        } else {
          if constexpr (f < 4) smF<3, 2, 20 + f>(c);
          else if constexpr (f < 16) smC<3, 2, f - 4>(c);
        }
        FENCE();
        mfo(IC<f>{}, IC<2>{});
        if constexpr (f + PD < 32) rdh(IC<f + PD>{});
        dsal(IC<f>{});
        FENCE();
      });
    });
  } else if constexpr (NB == 3) {
    if constexpr (FINAL) {
      sfor<20>([&](auto K) AI { smF<3, 2, K>(c); });
      sfor<9>([&](auto K) AI { smE<3, 2, K>(c); });
      FENCE();
    }
    sfor<2>([&](auto U_) AI {
      constexpr int u = U_;
      sfor<3>([&](auto Bk) AI {
        constexpr int blk = Bk;
        mfl(U_, Bk);
        if constexpr (!FINAL) {
          if constexpr (u == 0) { smF<3, 0, 2 * blk>(c); smF<3, 0, 2 * blk + 1>(c); }
          else if constexpr (blk < 2) smE<3, 0, 16 + blk>(c);
          else { smF<3, 0, 18>(c); smF<3, 0, 19>(c); }
        } else if constexpr (u == 0) smE<3, 2, 9 + blk>(c);
        FENCE();
      });
      sfor<16>([&](auto FF) AI {
        constexpr int f = 16 * u + FF, nd = f - ((f + 2) >> 2);
        constexpr bool dm = (f & 3) == 1;
        mfo(IC<f>{}, IC<0>{});
        if constexpr (!FINAL) {
          if constexpr (f < 16) smE<3, 0, f>(c);
          else if constexpr (f == 16) { smF<3, 0, 20>(c); smF<3, 0, 21>(c); }
          else if constexpr (f == 17) { smF<3, 0, 22>(c); smF<3, 0, 23>(c); }
          else if constexpr (f < 24) smE<3, 0, f>(c);
          else if constexpr (f == 24) { smC<3, 0, 6>(c); smC<3, 0, 7>(c); }
          else if constexpr (f == 25) { smC<3, 0, 9>(c); smC<3, 0, 10>(c); }
          else if constexpr (f == 26) smC<3, 0, 11>(c);
        } else {
          if constexpr (f < 12) smE<3, 2, 12 + f>(c);
          else if constexpr (f == 12) { smC<3, 2, 5>(c); smC<3, 2, 6>(c); }
          else if constexpr (f == 13) { smC<3, 2, 8>(c); smC<3, 2, 9>(c); }
          else if constexpr (f == 14) { smC<3, 2, 10>(c); smC<3, 2, 11>(c); }
        }
        FENCE();
        mfo(IC<f>{}, IC<1>{});
        if constexpr (f + PD < 32) { rdl(IC<f + PD>{}); if constexpr (dm) rdh(IC<f + PD>{}); }
        FENCE();
        if constexpr (!dm) {
          if constexpr (!FINAL) {
            if constexpr (f < 16) smF<3, 0, 6 + nd>(c);
            else if constexpr (f == 16) smC<3, 0, 0>(c);
            else if constexpr (f >= 18 && f <= 20) smC<3, 0, f - 17>(c);
            else if constexpr (f == 22 || f == 23) smC<3, 0, f - 18>(c);
            else if constexpr (f == 24) smC<3, 0, 8>(c);
          } else {
            if constexpr (f < 5) smF<3, 2, 20 + nd>(c);
            else if constexpr (f >= 6 && f <= 8) smC<3, 2, f - 6>(c);
            else if constexpr (f == 10 || f == 11) smC<3, 2, f - 7>(c);
            else if constexpr (f == 12) smC<3, 2, 7>(c);
          }
        }
        FENCE();
        mfo(IC<f>{}, IC<2>{});
        if constexpr (dm) dma_ld<(f >> 2), NT>(d);
        else if constexpr (f + PD < 32) rdh(IC<f + PD>{});
        dsal(IC<f>{});
        FENCE();
      });
    });
  } else {
    constexpr int NU = NB * 4, GH = 17 * NB;
    sfor<2>([&](auto U_) AI {
      constexpr int u = U_;
      sfor<17>([&](auto FF) AI {
        constexpr int ff = FF, f = 16 * u + ff - 1;
        sfor<NB>([&](auto Bk) AI {
          constexpr int blk = Bk, g = u * GH + ff * NB + blk;
          if constexpr (ff == 0) mfl(U_, Bk);
          else {
            mfo(IC<f>{}, Bk);
            if constexpr (f + PD < 32) { if constexpr (blk == 0) rdl(IC<f + PD>{}); else rdh(IC<f + PD>{}); }
            if constexpr (blk == 1 && (f & 3) == 1) dma_ld<(f >> 2), NT>(d);
            if constexpr (blk == 0) dsal(IC<f>{});
            FENCE();
          }
          if constexpr (FINAL) {
            constexpr int NOPS = NU * 5 + 5, GF = GH - 2;
            constexpr int lo = (g < GF) ? (g * NOPS) / GF : NOPS, hi = (g + 1 < GF) ? ((g + 1) * NOPS) / GF : NOPS;
            sfor<hi - lo>([&](auto J) AI { sm_gen<NB, 2, lo + J>(c); });
          } else {
            constexpr int G1 = 2 * GH - 4, NOPS = NU * 4;
            constexpr int lo = g < G1 ? (g * NOPS) / G1 : NOPS, hi = g + 1 < G1 ? ((g + 1) * NOPS) / G1 : NOPS;
            sfor<hi - lo>([&](auto J) AI { constexpr int k = lo + J; sm_gen<NB, 0, (k / 4) * 5 + (k % 4)>(c); });
            sfor<NU>([&](auto UU) AI {
              constexpr int un = UU;
              constexpr int gdone = ((4 * un + 4) * G1 + NOPS - 1) / NOPS + 1, gmin = GH + NB + un;
              constexpr int gat = gdone > gmin ? gdone : gmin;
              if constexpr (g == (gat < 2 * GH - 1 ? gat : 2 * GH - 1)) smC<NB, 0, un>(c);
            });
          }
          FENCE();
        });
      });
    });
  }
  if constexpr (!FINAL) {
    if (__builtin_expect(chg != 0ull, 0)) {
      asm volatile("s_nop 7" ::: "memory");
      FENCE();
      sfor<NB>([&](auto Bk) AI { constexpr int blk = Bk;
        const float f = __builtin_amdgcn_exp2f(-c.dl[blk]);
        c.L[blk] *= f;
        sfor<64>([&](auto Z) AI { acc_mul<OREG(blk, 0) + Z>(f); }); });
      FENCE();
      asm volatile("s_nop 3" ::: "memory");
    }
  }
}

constexpr int EPD = 4, ERG = EPD + 1, EFB = 32 - EPD;
constexpr unsigned WSL(int c) { return ((34 + c) % 5) * SLOT; }
template <int NB> struct Epi {
  bf16x8 of[NB][8];
  f32x4  y[2][NB][2];
  float  rl[NB];
  u32x4  t[2];
  bf16x4 flo[ERG], fhi[ERG];
  unsigned vP[NB], ob[4], vW, soW;
  u32x4  srdP, srdW;
};
constexpr int DOP = 13;
template <int NB, int BUF, int K> DI void drain_op(Epi<NB>& e, unsigned so) {
  constexpr int blk = K / DOP, pos = K % DOP;
  if constexpr (pos < 8) { const float v = e.y[BUF][blk][pos >> 2][pos & 3]; e.y[BUF][blk][pos >> 2][pos & 3] = mul_v(v, e.rl[blk]); }
  else if constexpr (pos < 12) { constexpr int q = pos - 8; e.t[blk & 1][q] = cvt_v(e.y[BUF][blk][q >> 1][2 * (q & 1)], e.y[BUF][blk][q >> 1][2 * (q & 1) + 1]); }
  else {
    st16_sc1(e.t[blk & 1], e.vP[blk], so, e.srdP);
  }
}
#define EPLAN 0
constexpr int e_dma(int P, int f) {
  if (EPLAN == 0) return P == 0 ? (f < 16 ? f : -1) : ((P <= 2 && (f & 3) == 1) ? 8 * (P + 1) + (f >> 2) : -1);
  if (EPLAN == 1) return P == 0 ? ((f & 1) ? (f >> 1) : -1) : ((P <= 2 && (f & 3) == 1) ? 8 * (P + 1) + (f >> 2) : -1);
  return P == 0 ? ((f < 16 && (f & 1)) ? (f >> 1) : -1) : (P == 1 ? ((f & 1) ? 8 + (f >> 1) : -1) : ((P == 2 && (f & 3) == 1) ? 24 + (f >> 2) : -1));
}
constexpr int e_pos(int v) { for (int G = 0; G < 128; ++G) if (e_dma(G / 32, G % 32) == v) return G; return -1; }
constexpr int e_dmp(int P, int f) { return f > 0 ? e_dma(P, f - 1) : (P > 0 ? e_dma(P - 1, 31) : -1); }
template <int NB> constexpr bool e_avail(int P, int f, int g) {
  if ((f & 15) == 0 && g == 0) return false;
  if (g == NB - 1 && e_dma(P, f) >= 0) return false;
  if (g == 0 && e_dmp(P, f) >= 0) return false;
  if (NB == 3 && g == 1 && (f & 7) == 8 - EPD) return false;
  return true;
}
template <int NB> constexpr int e_slot(int P, int f, int g) {
  int n = 0;
  for (int ff = f & ~15; ff < (f & ~15) + 16; ++ff) for (int gg = 0; gg < NB; ++gg) { if (ff == f && gg == g) return n; n += e_avail<NB>(P, ff, gg); }
  return n;
}
template <int NB> constexpr int e_lo(int P, int f, int g) { const int N = e_slot<NB>(P, f, NB); return (e_slot<NB>(P, f, g) * DOP * NB + N - 1) / N; }
template <int NB> constexpr int e_hi(int P, int f, int g) { const int N = e_slot<NB>(P, f, NB); return e_avail<NB>(P, f, g) ? ((e_slot<NB>(P, f, g) + 1) * DOP * NB + N - 1) / N : e_lo<NB>(P, f, g); }
template <int NB> constexpr int e_nst(int P, int f) {
  int n = 0;
  if (P == 0 && f < 16) return 0;
  for (int g = 0; g < NB; ++g) for (int k = e_lo<NB>(P, f, g); k < e_hi<NB>(P, f, g); ++k) n += k % DOP == DOP - 1;
  return n;
}
template <int NB> constexpr int e_stg(int P, int f, int g) {
  if (P == 0 && f < 16) return -1;
  for (int k = e_lo<NB>(P, f, g); k < e_hi<NB>(P, f, g); ++k) if (k % DOP == DOP - 1) return k / DOP;
  return -1;
}
template <int NB> constexpr int e_stp(int P, int f, int g) {
  return g > 0 ? e_stg<NB>(P, f, g - 1) : (f > 0 ? e_stg<NB>(P, f - 1, NB - 1) : (P > 0 ? e_stg<NB>(P - 1, 31, NB - 1) : -1));
}
template <int NB> constexpr int e_lastst(int P) { int r = 0; for (int f = 0; f < 16; ++f) if (e_nst<NB>(P, f)) r = f; return r; }
template <int NB> constexpr int e_cnt(int P0, int f0, int P1, int f1) {
  int n = 0;
  for (int G = 32 * P0 + f0 + 1; G < 32 * P1 + f1; ++G) n += (e_dma(G / 32, G % 32) >= 0) + e_nst<NB>(G / 32, G % 32);
  return n;
}
template <int NB, int G> DI void erdl(Epi<NB>& e) { constexpr int f = G & 31; e.flo[G % ERG] = lds_tr(e.ob[f >> 3] + 4096 * (f & 7)); }
template <int NB, int G> DI void erdh(Epi<NB>& e) { constexpr int f = G & 31; e.fhi[G % ERG] = lds_tr(e.ob[f >> 3] + 4096 * (f & 7) + 2048); }
template <int NB, int P, class MID> DI void phaseP(Epi<NB>& e, unsigned mb4, MID&& mid) {
  sfor<32>([&](auto F_) AI {
    constexpr int f = F_, G = 32 * P + f, HF = f >> 4, nb = (f >> 3) & 1, s = f & 7;
    constexpr bool nxt = P < 6 || f + EPD < 32;
    if constexpr (f == EFB && P == 0) wait_bar<8 + e_cnt<NB>(0, -1, 0, EFB)>();
    if constexpr (f == EFB && P == 1) wait_bar<e_cnt<NB>(0, e_pos(7), 1, EFB)>();
    if constexpr (f == EFB && P == 3) asm volatile("s_waitcnt vmcnt(%0)\n\ts_barrier" :: "i"(e_cnt<NB>(0, e_pos(31), 3, EFB) + 1) : "memory", XCLOB);
    if constexpr (f == 16) mid();
    constexpr int v = e_dma(P, f), vp = e_dmp(P, f);
    sfor<NB>([&](auto Bk) AI {
      constexpr int blk = Bk;
      const bf16x8 a = cat8(e.flo[G % ERG], e.fhi[G % ERG]);
      if constexpr (s == 0) mf_vv0(e.y[HF][blk][nb], a, e.of[blk][s]); else mf_vv(e.y[HF][blk][nb], a, e.of[blk][s]);
      if constexpr (blk == 0 && nxt) erdl<NB, G + EPD>(e);
      if constexpr (blk == 1 && nxt) erdh<NB, G + EPD>(e);
      FENCE();
      if constexpr (blk == 0) {
        if constexpr (vp >= 0) sadd_ip<(vp & 7) == 7 ? 128 - 133120 : ((vp & 3) == 3 ? 96256 : 6144)>(e.soW);
        if constexpr (v >= 0 && (v & 3) == 0) m0_set<(int)WSL(3 + v / 8) + 16384 * ((v & 7) >> 2)>(mb4);
      }
      if constexpr (blk == 1 && (f & 7) == 8 - EPD && P < 6) vadd_ip<(int)WSL(P + 1) - (int)WSL(P)>(e.ob[f >> 3]);
      if constexpr (!(P == 0 && HF == 0))
        sfor<e_hi<NB>(P, f, blk) - e_lo<NB>(P, f, blk)>([&](auto J) AI { drain_op<NB, HF ^ 1, e_lo<NB>(P, f, blk) + J>(e, (unsigned)(2 * P - 1 + HF) * 1024u); });
      if constexpr (blk == NB - 1 && v >= 0) {
        if constexpr (NTE) asm volatile("buffer_load_dwordx4 %0, %1, %2 offen offset:%3 nt lds" :: "v"(e.vW), "s"(e.srdW), "s"(e.soW), "i"(1024 * (v & 3)));
        else               asm volatile("buffer_load_dwordx4 %0, %1, %2 offen offset:%3 lds"    :: "v"(e.vW), "s"(e.srdW), "s"(e.soW), "i"(1024 * (v & 3)));
      }
      if constexpr (e_stp<NB>(P, f, blk) >= 0) asm volatile("" :: "v"(e.t[e_stp<NB>(P, f, blk) & 1]));
      FENCE();
    });
  });
}

struct Red {
  u32x4 srdF, srdR, srdO;
  unsigned tlo, thi;
  unsigned vF;
  unsigned vR, vO;
  unsigned lane;
};
DI void flag_set(const Red& r, unsigned line) {
  u32x2 tg = {r.tlo, r.thi};
  st8_sc1(tg, r.vF, line * 128u, r.srdF);
}
DI void poll_issue(const Red& r, unsigned line) {
  const unsigned vo = r.lane < 16u ? line * 128u + r.lane * 8u : OOB;
  asm volatile("buffer_load_dwordx2 a[126:127], %0, %1, 0 offen sc1" :: "v"(vo), "s"(r.srdF) : "memory", XCLOB);
}
DI bool poll_ok(const Red& r) {
  unsigned lo, hi;
  asm volatile("v_accvgpr_read_b32 %0, a126\n\tv_accvgpr_read_b32 %1, a127" : "=v"(lo), "=v"(hi) :: XCLOB);
  return __ballot(r.lane >= 16u || (lo == r.tlo && hi == r.thi)) == ~0ull;
}
DI void flag_wait(const Red& r, unsigned line) {
#pragma unroll 1
  for (int it = 0; it < (1 << 22); ++it) {
    poll_issue(r, line);
    wait_vm<0>();
    if (poll_ok(r)) break;
    __builtin_amdgcn_s_sleep(1);
  }
}
DI void poll2_issue(const Red& r, unsigned lineA, unsigned lineB) {
  const unsigned vo = r.lane < 32u ? (r.lane < 16u ? lineA : lineB) * 128u + (r.lane & 15u) * 8u : OOB;
  asm volatile("buffer_load_dwordx2 a[126:127], %0, %1, 0 offen sc1" :: "v"(vo), "s"(r.srdF) : "memory", XCLOB);
}
DI bool poll2_ok(const Red& r) {
  unsigned lo, hi;
  asm volatile("v_accvgpr_read_b32 %0, a126\n\tv_accvgpr_read_b32 %1, a127" : "=v"(lo), "=v"(hi) :: XCLOB);
  return __ballot(r.lane >= 32u || (lo == r.tlo && hi == r.thi)) == ~0ull;
}
DI void flag_wait2(const Red& r, unsigned lineA, unsigned lineB) {
#pragma unroll 1
  for (int it = 0; it < (1 << 22); ++it) {
    poll2_issue(r, lineA, lineB);
    wait_vm<0>();
    if (poll2_ok(r)) break;
    __builtin_amdgcn_s_sleep(1);
  }
}
template <int R, int OFF> DI void pa_load(unsigned vo, unsigned so, const u32x4& srd) {
  asm volatile("buffer_load_dwordx4 a[%c0:%c1], %2, %3, %4 offen offset:%5 sc1" NTR :: "i"(R), "i"(R + 3), "v"(vo), "s"(srd), "s"(so), "i"(OFF) : XCLOB); }
template <int R> DI void red_issue(const Red& r, unsigned c, unsigned idx) {
  const unsigned fr = (idx >> 1) * 14u + 2u * c + (idx & 1u);
  const unsigned so = rfl(fr * 1024u);
  sfor<8>([&](auto J) AI {
    const unsigned s2 = so + 2u * J * PARTB;
    pa_load<R + 8 * J, 0>(r.vR, s2, r.srdR);
    pa_load<R + 8 * J + 4, 256>(r.vR, s2, r.srdR);
  });
}
template <int R> DI void red_finish(const Red& r, const bf16x8& sel, unsigned c, unsigned idx) {
  f32x4 a0, a1;
  asm volatile("" ::: XCLOB);
  sfor<8>([&](auto J) AI {
    if constexpr (J == 0) { mf_vx0<R>(a0, sel); mf_vx0<R + 4>(a1, sel); }
    else { mf_vx<R + 8 * J>(a0, sel); mf_vx<R + 8 * J + 4>(a1, sel); }
  });
  asm volatile("s_nop 7\n\ts_nop 1" ::: "memory", XCLOB);
  FENCE();
  u32x4 t = {cvt_v(a0[0], a0[1]), cvt_v(a0[2], a0[3]), cvt_v(a1[0], a1[1]), cvt_v(a1[2], a1[3])};
  const unsigned so = rfl((idx >> 1) * (16u * 2u * DM * 2u) + (2u * c + (idx & 1u)) * 64u);
  st16(t, r.vO, so, r.srdO);
  asm volatile("s_nop 1");
}

template <int NB>
DI void body(const __hip_bfloat16* __restrict__ x, const __hip_bfloat16* __restrict__ kc, const __hip_bfloat16* __restrict__ vc,
             const __hip_bfloat16* __restrict__ wq, const __hip_bfloat16* __restrict__ wo, char* __restrict__ ws, __hip_bfloat16* __restrict__ out,
             int br, int b, int h, unsigned jx, unsigned w, unsigned lane, unsigned lds0, uint64_t pc0) {
  const int grp = br * 8 + b;
  const unsigned g = lane >> 4, cc = lane & 15u, q4 = cc >> 2, p4 = lane & 3u, hi5 = lane >> 5, ch = lane & 31u;
  Ctx<NB> c;
  const u32x4 srdK = mk_srd((const char*)kc + ((size_t)(b * NH + h) * SK) * (DH * 2), SK * DH * 2);
  const u32x4 srdV = mk_srd((const char*)vc + ((size_t)(b * NH + h) * SK) * (DH * 2), SK * DH * 2);
  const u32x4 srdWq = mk_srd((const char*)wq + h * (DH * 2), DM * NH * DH * 2 - h * (DH * 2));
  const u32x4 srdWo = mk_srd((const char*)wo + (size_t)h * DH * DM * 2, DH * DM * 2);
  const u32x4 srdX = mk_srd((const char*)x + (size_t)b * SQ * DM * 2, SQ * DM * 2);
  const u32x4 srdF = mk_srd(ws + FLAG_OFF + (size_t)grp * 4096, 4096);
  const unsigned rb[3] = {2u * w, 2u * w + 1u, 8u + w};
  Prof pf;
  POST(0); RT(0);

  { const unsigned ve = 28u * 128u; asm volatile("buffer_load_dwordx2 a[250:251], %0, %1, 0 offen sc1" :: "v"(ve), "s"(srdF) : XCLOB); }
  const unsigned mb4 = rfl(lds0 + 4096u * w);
  Desc dQ;
  dQ.srd = srdWq; dQ.inc = 15360u; dQ.ginc = 216064u;
  dQ.v[0] = dQ.v[2] = 8192u * hi5 + 16u * (ch ^ (2u * (hi5 + 4u * (w & 1u)))); dQ.v[1] = dQ.v[3] = dQ.v[0] ^ 64u;
  const unsigned soQ = rfl(65536u * w);
  dQ.mb = mb4 + QSL(0); dQ.so = soQ;
  sfor<8>([&](auto I) AI { dma_piece<I, NTW>(dQ); });
  unsigned vx[NB];
  sfor<NB>([&](auto Bk) AI { vx[Bk] = (16u * rb[Bk] + (lane >> 2)) * (DM * 2) + 16u * (p4 ^ (2u * hi5)); });
  auto xld = [&](auto K_) AI { constexpr int k = K_, ks = k / NB, bk = k % NB; xa_load<XREG<NB>(ks, bk), 64 * ks>(vx[bk], srdX); };
  POST(53);
  sfor<4 * NB>(xld);
  dQ.mb = mb4 + QSL(1); dQ.so = soQ + 64u * 8192u;
  sfor<8>([&](auto I) AI { dma_piece<I, NTW>(dQ); });
  POST(54);
  sfor<2 * NB>([&](auto K) AI { xld(IC<4 * NB + K>{}); });
  const unsigned vlin = lane * 16u;
#define PFQ 0
#define PFNT ""
  auto pfq = [&](unsigned vo, unsigned so, const u32x4& srd) AI { asm volatile("buffer_load_dwordx4 a[252:255], %0, %1, %2 offen" PFNT :: "v"(vo), "s"(srd), "s"(so)); };
  constexpr int NPF = PFQ ? 10 : 0;
  if constexpr (PFQ) {
    const unsigned vq = (16u * w + 8u * hi5 + (unsigned)b) * 8192u + ch * 16u;
    const unsigned vxp = (40u * (jx & 3u) + 10u * w + hi5) * (DM * 2) + 384u + ch * 16u;
    sfor<5>([&](auto M) AI { pfq(vq, (M + 2u) * 524288u, srdWq); pfq(vxp, M * (4u * DM), srdX); });
  }
  if constexpr (NB == 2) {
    if (w == 3) {
      const u32x4 srdC = mk_srd((const void*)(pc0 & ~127ull), CODE_PF);
      const unsigned so = rfl(jx * (CODE_PF / 32u));
      pf4_keep<0>(vlin, so, srdC); pf4_keep<1024>(vlin, so, srdC); pf4_keep<2048>(vlin, so, srdC);
    }
  }
  PRE(1);
  c.kbase = lds0 + 512u * cc + 16u * (g ^ (cc & 3u)) + 64u * (cc >> 2);
  c.vbase = lds0 + 512u * (4u * g + q4) + 32u * (4u * (g & 1u) + q4) + 8u * p4;
  { u32x4 o = {0x3F803F80u, 0x3F803F80u, 0x3F803F80u, 0x3F803F80u}; c.ones = __builtin_bit_cast(bf16x8, o); tie_v8(c.ones); }
  Desc dA, dB;
  dA.srd = srdV; dB.srd = srdK; dA.inc = dB.inc = 0u; dA.ginc = dB.ginc = 16384u;
  sfor<4>([&](auto K) AI { dB.v[K] = 512u * hi5 + 16u * (ch ^ (8u * (w & 1u) + 2u * K + hi5)); dA.v[K] = 512u * hi5 + 16u * (ch ^ (2u * (2u * K + hi5))); });

  unsigned tlo, thi;
  {
    f32x4 qa[NB][16];
    u32x4 qt[NB][8];
    const unsigned wbase = lds0 + 512u * (8u * g + q4) + 32u * (q4 + 4u * (g & 1u)) + 8u * p4;
    const unsigned pa = 4u * (4u * cc + (g ^ (2u * (cc >> 3))));
    auto qcv = [&](auto BB, auto SP, auto K_) AI {
      constexpr int bb = BB, sp = SP, k = K_;
      if constexpr (k < 4) qt[bb][sp][k] = cvt_v(qa[bb][2 * sp + (k >> 1)][2 * (k & 1)], qa[bb][2 * sp + (k >> 1)][2 * (k & 1) + 1]);
      else { unsigned x0 = qt[bb][sp][k & 1], x1 = qt[bb][sp][2 + (k & 1)];
        if constexpr (k < 6) swap32(x0, x1); else swap16(x0, x1);
        qt[bb][sp][k & 1] = x0; qt[bb][sp][2 + (k & 1)] = x1; }
    };
    constexpr int PD = QPD, RG = PD + 1, FB = QFB, RING = NB == 3 ? 120 : 160;
    constexpr QSched<NB> qs{};
    unsigned wb[8];
    sfor<8>([&](auto J) AI { wb[J] = (wbase ^ (32u * J)) + QSL(0); tie_v(wb[J]); });
    auto rdl = [&](auto F) AI { constexpr int f = F & 31, be = f >> 1, u = f & 1; tr_acc<RING + 4 * (F % RG), 256 * (be >> 3) + 16384 * u>(wb[be & 7]); };
    auto rdh = [&](auto F) AI { constexpr int f = F & 31, be = f >> 1, u = f & 1; tr_acc<RING + 4 * (F % RG) + 2, 256 * (be >> 3) + 16384 * u + 2048>(wb[be & 7]); };
    auto xbp = [&](auto C_, auto K) AI { constexpr int C = C_, k = K; xa_perm<XREG<NB>(2 * C + k / (4 * NB), 0) + k % (4 * NB)>(pa); };
    auto kv_pre = [&](auto J) AI { constexpr int j = J, cn = j >> 3, I = j & 7;
      if constexpr (I == 0) { if constexpr (cn < 2) { dB.mb = mb4 + (2 + cn) * SLOT; dB.so = cn * SLOT + 4096u * w; } else { dA.mb = mb4 + 4u * SLOT; dA.so = 4096u * w; } }
      if constexpr ((I & 3) == 0) { if constexpr (cn < 2) dma_m0<I>(dB); else dma_m0<I>(dA); } };
    auto kv_op = [&](auto J) AI { constexpr int j = J, cn = j >> 3, I = j & 7; if constexpr (cn < 2) dma_ld<I, true>(dB); else dma_ld<I, true>(dA); };
    auto kv_post = [&](auto J) AI { constexpr int j = J, cn = j >> 3, I = j & 7; if constexpr (cn < 2) dma_adv<I>(dB); else dma_adv<I>(dA); };
    auto vm_pre = [&](auto C_, auto V_) AI {
      constexpr int C = C_, v = V_, k = v - q_nvw(C) - q_nvx<NB>(C);
      if constexpr (v < q_nvw(C)) { if constexpr ((v & 3) == 0) dma_m0<v>(dQ); }
      else if constexpr (k >= 0) kv_pre(IC<q_kv0(C) + k>{});
    };
    auto vm_op = [&](auto C_, auto V_) AI {
      constexpr int C = C_, v = V_, xk = v - q_nvw(C), k = xk - q_nvx<NB>(C);
      if constexpr (v < q_nvw(C)) dma_ld<v, NTW>(dQ);
      else if constexpr (k < 0) xld(IC<(6 + 2 * C) * NB + xk>{});
      else kv_op(IC<q_kv0(C) + k>{});
    };
    auto vm_post = [&](auto C_, auto V_) AI {
      constexpr int C = C_, v = V_, k = v - q_nvw(C) - q_nvx<NB>(C);
      if constexpr (v < q_nvw(C)) dma_adv<v>(dQ);
      else if constexpr (k >= 0) kv_post(IC<q_kv0(C) + k>{});
    };
    constexpr int WT[7] = {8 + 2 * NB + NPF, 8 + 2 * NB + NPF, 8 + 2 * NB, 8 + 2 * NB, 8 + 2 * NB, 12, 12};
    PRE(2);
    wait_vm<WT[0]>();
    tlo = rfl(__float_as_uint(acc_rd<250>())) + 1u; thi = rfl(__float_as_uint(acc_rd<251>())) + (tlo == 0u ? 1u : 0u);
    sfor<8 * NB>([&](auto K) AI { xbp(IC<0>{}, K); });
    asm volatile("s_waitcnt lgkmcnt(0)\n\ts_barrier" ::: "memory", XCLOB);
    POST(2); RT(2);
    sfor<PD>([&](auto F) AI { rdl(F); rdh(F); });
    sfor<7>([&](auto C_) AI {
      constexpr int C = C_;
      if constexpr (C <= 4) { dQ.mb = mb4 + QSL(C + 2); dQ.so = soQ + 64u * 8192u * (C + 2); }
      vm_pre(C_, IC<0>{});
      FENCE();
      sfor<32>([&](auto F_) AI {
        constexpr int f = F_, F = 32 * C + f, be = f >> 1, u = f & 1;
        constexpr bool nxt = C < 6 || f + PD < 32;
        if constexpr (f == FB && C < 6) {
          PRE(3 + C);
          asm volatile("s_waitcnt vmcnt(%0) lgkmcnt(0)\n\ts_barrier" :: "i"(WT[C + 1]) : "memory", XCLOB);
          POST(3 + C); RT(3 + C);
          FENCE();
        }
        if constexpr (qs.wait[F] >= 0) wait_lgkm<(qs.wait[F] > 15 ? 15 : qs.wait[F])>();
        auto mf = [&](auto Bk) AI { constexpr int blk = Bk, XR = XREG<NB>(2 * C + u, blk), RA = RING + 4 * (F % RG);
          if constexpr (C == 0 && u == 0) mf_xx0<RA, XR>(qa[blk][be]); else mf_xx<RA, XR>(qa[blk][be]);
          FENCE(); };
        auto wbn = [&]() AI { if constexpr (C < 6 && f >= 13 && ((f - 13) & 1) == 0 && (f - 13) / 2 < 8) {
          constexpr int j = (f - 13) / 2; vadd_ip<(int)(QSL(C + 1) - QSL(C))>(wb[j]); } };
        auto cv = [&](auto K) AI { constexpr int k = K, un = k >> 3, sp2 = un / NB, bb = un % NB; if constexpr (k >= 0 && sp2 < 8) qcv(IC<bb>{}, IC<sp2>{}, IC<(k & 7)>{}); };
        constexpr int v = q_vat<NB>(C, f), vn = q_vat<NB>(C, f + 1);
        constexpr int k0 = q_cv0<NB>(f);
        if constexpr (NB == 3) {
          mf(IC<0>{});
          if constexpr (v >= 0) vm_op(C_, IC<v>{});
          else if constexpr (q_bpa<NB>(C, f)) xbp(IC<C + 1>{}, IC<f / 2>{});
          else if constexpr (C == 6 && f >= 6) { cv(IC<k0>{}); cv(IC<k0 + 1>{}); }
          FENCE();
          mf(IC<1>{});
          if constexpr (nxt) rdl(IC<F + PD>{});
          FENCE();
          if constexpr (C < 6) wbn(); else if constexpr (f >= 6) cv(IC<k0 + (v >= 0 ? 0 : 2)>{});
          FENCE();
          mf(IC<2>{});
          if constexpr (nxt) rdh(IC<F + PD>{});
          if constexpr (q_bpb<NB>(C, f)) xbp(IC<C + 1>{}, IC<13 + f / 2>{});
          if constexpr (v >= 0) vm_post(C_, IC<v>{});
          if constexpr (vn >= 0) vm_pre(C_, IC<vn>{});
          FENCE();
        } else {
          mf(IC<0>{});
          if constexpr (nxt) rdl(IC<F + PD>{});
          if constexpr (q_bpa<NB>(C, f)) xbp(IC<C + 1>{}, IC<f>{});
          if constexpr (C == 6 && f >= 6) { cv(IC<k0>{}); cv(IC<k0 + 1>{}); }
          FENCE();
          wbn();
          FENCE();
          mf(IC<1>{});
          if constexpr (nxt) rdh(IC<F + PD>{});
          if constexpr (v >= 0) { vm_op(C_, IC<v>{}); vm_post(C_, IC<v>{}); }
          if constexpr (vn >= 0) vm_pre(C_, IC<vn>{});
          if constexpr (C == 6 && f >= 6) { cv(IC<k0 + 2>{}); cv(IC<k0 + 3>{}); }
          FENCE();
        }
      });
    });
    PRE(9);
    kv_pre(IC<20>{});
    asm volatile("s_nop 7" ::: "memory", XCLOB);
    FENCE();
    constexpr int DONE = q_cv0<NB>(32) / 8;
    constexpr int REST0 = q_cv0<NB>(32) % 8;
    sfor<8 - REST0>([&](auto K) AI { constexpr int un = DONE, sp2 = un / NB, bb = un % NB; if constexpr (REST0 != 0) { qcv(IC<bb>{}, IC<sp2>{}, IC<REST0 + K>{}); asm volatile("s_nop 1"); } });
    constexpr int U0 = DONE + (REST0 != 0 ? 1 : 0), NU = 8 * NB - U0;
    sfor<8>([&](auto Q_) AI { constexpr int q = Q_;
      if constexpr ((q & 1) == 0) { kv_op(IC<20 + q / 2>{}); kv_post(IC<20 + q / 2>{}); }
      sfor<NU>([&](auto U_) AI { constexpr int un = U0 + U_, sp2 = un / NB, bb = un % NB; qcv(IC<bb>{}, IC<sp2>{}, Q_); }); });
    sfor<NB - 1>([&](auto Bk) AI { sfor<8>([&](auto Sp) AI { c.Q[Bk + 1][Sp] = __builtin_bit_cast(bf16x8, qt[Bk + 1][Sp]); }); });
    sfor<32>([&](auto K) AI { acc_wr<QREG<NB>(0) + K>(qt[0][K / 4][K % 4]); });
    FENCE();
    asm volatile("s_nop 1" ::: "memory");
  }

  {
    dB.mb = mb4; dB.so = 2u * SLOT + 4096u * w;
    phaseA<NB, true, true>(c, 2u * SLOT, dB, pf, 10);
    PRE(11);
    phaseB0<NB>(c);
  }
  unsigned sl = 3u * SLOT, pv = 2u * SLOT, pv2 = SLOT;
  auto next = [&]() AI { pv2 = pv; pv = sl; sl = sl == 4u * SLOT ? 0u : sl + SLOT; };
  unsigned soA = SLOT + 4096u * w, soB = 3u * SLOT + 4096u * w, stepB = SLOT;
  for (int t = 1; t < 16; ++t) {
    if (t == 15) {
      dA.srd = srdWo; dA.inc = 6144u; dA.ginc = 96256u; soA = 28672u * w;
      const unsigned l = lane_id(), vWo = 896u * (l >> 3) + 16u * ((l & 7u) ^ (2u * ((l >> 4) & 3u)));
      sfor<4>([&](auto K) AI { dA.v[K] = mov_v(vWo); });
      const unsigned vo = (32u * (unsigned)b + 8u * w + (l >> 5)) * (DM * 2) + 384u + (l & 31u) * 16u;
      pf4_load<0>(vo, 0u, srdWo); pf4_load<2 * DM * 2>(vo, 0u, srdWo); pf4_load<0>(vo, 4u * DM * 2, srdWo); pf4_load<2 * DM * 2>(vo, 4u * DM * 2, srdWo);
    }
    dA.mb = mb4 + pv2; dA.so = soA; soA += SLOT;
    phaseA<NB, false, true>(c, sl, dA, pf, 10 + 2 * t);
    next();
    if (t >= 14) {
      dB.srd = dA.srd; dB.inc = dA.inc; dB.ginc = dA.ginc; soB = t == 14 ? 15u * SLOT + 4096u * w : 28672u * w + 128u;
      sfor<4>([&](auto K) AI { dB.v[K] = mov_v(dA.v[K]); });
    }
    dB.mb = mb4 + pv2; dB.so = soB; soB += SLOT;
    phaseB<NB, false, true>(c, sl, dB, pf, 11 + 2 * t);
    next();
  }
  dB.mb = mb4 + pv2; dB.so = 28672u * w + 256u;
  phaseB<NB, true, true>(c, sl, dB, pf, 42);
  next();
  PRE(43); wait_bar<16>(); POST(43);

  Epi<NB> e;
  const unsigned laneE = lane_id(), gE = laneE >> 4, ccE = laneE & 15u, q4E = ccE >> 2;
  {
    const unsigned obase = lds0 + WSL(0) + 128u * (4u * gE + q4E) + 32u * ((2u * gE + (q4E >> 1)) & 3u) + 8u * (laneE & 3u);
    sfor<4>([&](auto Nb) AI { e.ob[Nb] = obase ^ (32u * Nb); tie_v(e.ob[Nb]); });
  }
  sfor<EPD>([&](auto G) AI { erdl<NB, G>(e); erdh<NB, G>(e); });
  e.vW = dB.v[0]; e.srdW = srdWo; e.soW = rfl(28672u * w + 384u);
  asm volatile("s_nop 7" ::: "memory");
  FENCE();
  sfor<NB>([&](auto Bk) AI { constexpr int blk = Bk;
    tie_v4(c.L[blk]);
    e.rl[blk] = __builtin_amdgcn_rcpf(c.L[blk][0]);
    sfor<8>([&](auto Sp) AI { constexpr int sp = Sp;
      constexpr int R = OREG(blk, 2 * sp);
      const float a0 = acc_rd<R>(), a1 = acc_rd<R + 1>(), a2 = acc_rd<R + 2>(), a3 = acc_rd<R + 3>();
      const float a4 = acc_rd<R + 4>(), a5 = acc_rd<R + 5>(), a6 = acc_rd<R + 6>(), a7 = acc_rd<R + 7>();
      u32x4 t = {cvt_v(a0, a1), cvt_v(a2, a3), cvt_v(a4, a5), cvt_v(a6, a7)};
      e.of[blk][sp] = __builtin_bit_cast(bf16x8, t); }); });
  FENCE();
  asm volatile("s_nop 1" ::: "memory");
  e.srdP = mk_srd(ws + (size_t)(grp * NH + h) * PARTB, PARTB);
  sfor<NB>([&](auto Bk) AI { e.vP[Bk] = rb[Bk] * (14u * 1024u) + laneE * 16u; });
  Red r;
  r.srdF = srdF; r.tlo = tlo; r.thi = thi; r.lane = laneE;
  r.vF = laneE == 0u ? (unsigned)h * 8u : OOB;
  r.srdR = mk_srd(ws + (size_t)grp * NH * PARTB, NH * PARTB);
  r.srdO = mk_srd((char*)out + ((size_t)b * SQ * 2 * DM + br * DM) * 2, (SQ * 2 * DM - br * DM) * 2);
  r.vR = (gE & 1u) * PARTB + (32u * (gE >> 1) + ccE) * 16u;
  r.vO = ccE * (2u * DM * 2u) + 32u * (gE & 1u) + 16u * (gE >> 1);

  asm volatile("; EPI_BEGIN" ::: XCLOB);
  u32x4 sv; sfor<4>([&](auto I) AI { constexpr int i = I; const unsigned m7 = ccE & 7u; const bool on = (gE >> 1) == (ccE >> 3);
    sv[i] = (on && m7 == 2u * i) ? 0x00003F80u : ((on && m7 == 2u * i + 1u) ? 0x3F800000u : 0u); });
  bf16x8 sel = __builtin_bit_cast(bf16x8, sv); tie_v8(sel);
  const unsigned c0 = (unsigned)h >> 2, ib = 16u + ((unsigned)h & 3u);
  const unsigned lB = (ib >> 1) - 8u;
  const unsigned e1c = w == 2u ? 0u : 2u, e2c = w == 2u ? 1u : c0, e2i = w == 2u ? (unsigned)h : ib;
  const bool e2on = w == 2u || c0 <= 2u;
  const unsigned l1 = e1c * 4u + c0, l2 = w == 2u ? 4u + c0 : (c0 <= 2u ? c0 * 4u + lB : l1);
  bool early = false;
  POST(44); RT(44);
  FENCE();
  sfor<7>([&](auto P_) AI {
    constexpr int p = P_;
    phaseP<NB, p>(e, mb4, [&]() AI {
      if constexpr (NB == 2 && p >= 5) {
        constexpr int bq = e_cnt<NB>(p - 1, 15, p, 16);
        if (p == 6 && w == 2u && early) wait_vm<bq + 32>(); else if (w == (unsigned)(p - 3)) wait_vm<bq>(); else if (p == 5) wait_vm<e_cnt<NB>(p - 1, e_lastst<NB>(p - 1), p, 16) + 1>(); else wait_vm<bq>();
      } else if constexpr (p >= 2) wait_vm<e_cnt<NB>(p - 1, e_lastst<NB>(p - 1), p, 16) + (p >= 3 ? 1 : 0)>();
      if constexpr (p >= 2) flag_set(r, (unsigned)(p - 2) * 4u + w);
      if constexpr (NB == 2 && p >= 4) {
        if (p >= 5 && !early && (p == 6 || w == 2u) && poll2_ok(r)) {
          early = true;
          red_issue<128>(r, e1c, h);
          if (e2on) red_issue<192>(r, e2c, e2i);
        }
        if (p <= 5 && !early && (p == 5 || w == 2u)) poll2_issue(r, l1, l2);
      }
      FENCE();
    });
    POST(45 + p); RT(45 + p);
  });
  {
    asm volatile("s_nop 7" ::: "memory");
    FENCE();
    sfor<NB * DOP>([&](auto K) AI { drain_op<NB, 1, K>(e, 13u * 1024u); if constexpr (K % DOP == DOP - 1) asm volatile("s_nop 1"); });
  }
  if constexpr (NB == 2) {
    const unsigned cT = w + 1u, cL = w + 3u;
    poll_issue(r, cT * 4u + c0);
    wait_vm<2 * NB + 1>();
    flag_set(r, 5u * 4u + w);
    wait_vm<1>();
    flag_set(r, 6u * 4u + w);
    POST(52); RT(52);
    bool okT = poll_ok(r);
    if (!early) {
      flag_wait2(r, l1, l2);
      red_issue<128>(r, e1c, h);
      if (e2on) red_issue<192>(r, e2c, e2i);
    }
    if (!okT) flag_wait(r, cT * 4u + c0);
    red_issue<0>(r, cT, h);
    if (!early) wait_vm<16>();
    red_finish<128>(r, sel, e1c, h);
    if (e2on) red_finish<192>(r, sel, e2c, e2i);
    poll_issue(r, cL * 4u + c0);
    wait_vm<2>();
    red_finish<0>(r, sel, cT, h);
    wait_vm<1>();
    POST(55); RT(55);
    if (!poll_ok(r)) flag_wait(r, cL * 4u + c0);
    red_issue<128>(r, cL, h);
    POST(56); RT(56);
    if (w == 2u && h == 0) { u32x2 tg = {tlo, thi}; st8_sc1(tg, laneE == 0u ? 0u : OOB, 28u * 128u, srdF); wait_vm<1>(); }
    else wait_vm<0>();
    red_finish<128>(r, sel, cL, h);
  } else {
    wait_vm<2 * NB>();
    flag_set(r, 5u * 4u + w);
    wait_vm<1>();
    flag_set(r, 6u * 4u + w);
    POST(52); RT(52);
    if (w == 0u) {
      const unsigned cB = c0 == 3u ? 3u : c0 + 4u;
      flag_wait(r, cB * 4u + lB); red_issue<128>(r, cB, ib);
      wait_vm<0>();
      red_finish<128>(r, sel, cB, ib);
    }
  }
}

__global__ __launch_bounds__(256, 1)
void attn_kernel(const __hip_bfloat16* __restrict__ xA, const __hip_bfloat16* __restrict__ kA, const __hip_bfloat16* __restrict__ vA,
                 const __hip_bfloat16* __restrict__ wqA, const __hip_bfloat16* __restrict__ woA,
                 const __hip_bfloat16* __restrict__ xB, const __hip_bfloat16* __restrict__ kB, const __hip_bfloat16* __restrict__ vB,
                 const __hip_bfloat16* __restrict__ wqB, const __hip_bfloat16* __restrict__ woB, char* __restrict__ ws, __hip_bfloat16* __restrict__ out) {
  extern __shared__ __attribute__((aligned(16))) char lds[];
  uint64_t pc0; asm volatile("s_getpc_b64 %0" : "=s"(pc0));
  const unsigned blk = blockIdx.x, xcd = blk & 7u, j = blk >> 3;
  const int br = xcd & 1u, h = (xcd >> 1) * 4 + (j & 3u), b = j >> 2;
  const unsigned lane = threadIdx.x & 63u, w = rfl(threadIdx.x >> 6);
  const unsigned lds0 = (unsigned)(size_t)lds;
  const __hip_bfloat16* x  = br ? xB : xA;
  const __hip_bfloat16* kc = br ? kB : kA;
  const __hip_bfloat16* vc = br ? vB : vA;
  const __hip_bfloat16* wq = br ? wqB : wqA;
  const __hip_bfloat16* wo = br ? woB : woA;
  if (w < 2) body<3>(x, kc, vc, wq, wo, ws, out, br, b, h, j, w, lane, lds0, pc0);
  else       body<2>(x, kc, vc, wq, wo, ws, out, br, b, h, j, w, lane, lds0, pc0);
}

extern "C" void kernel_launch(void* const* d_in, const int* in_sizes, int n_in, void* d_out, int out_size, void* d_ws, size_t ws_size,
                              hipStream_t stream) {
  (void)in_sizes; (void)n_in; (void)out_size; (void)ws_size;
  typedef const __hip_bfloat16* P;
  (void)hipFuncSetAttribute((const void*)attn_kernel, hipFuncAttributeMaxDynamicSharedMemorySize, 163840);
  attn_kernel<<<256, 256, 163840, stream>>>((P)d_in[0], (P)d_in[1], (P)d_in[2], (P)d_in[3], (P)d_in[4],
                                            (P)d_in[5], (P)d_in[6], (P)d_in[7], (P)d_in[8], (P)d_in[9], (char*)d_ws, (__hip_bfloat16*)d_out);
}
```
